```python
import torch

def _softmax(x, axis=-1):
    return torch.softmax(x, dim=axis)

def _gelu(x):
    return torch.nn.functional.gelu(x, approximate='tanh')

def _silu(x):
    return torch.nn.functional.silu(x)

def _sigmoid(x):
    return torch.sigmoid(x)

def _layernorm(x, axis=-1, eps=1e-05):
    m = x.mean(dim=axis, keepdim=True)
    v = x.var(dim=axis, keepdim=True, unbiased=False)
    return (x - m) / torch.sqrt(v + eps)

def _scatter_add(tgt, idx, src, axis=0):
    return tgt.clone().index_add_(axis, idx, src)

def _segment_sum(src, seg, n):
    out = torch.zeros((n,) + src.shape[1:], dtype=src.dtype)
    return out.index_add_(0, seg, src)

def _pad(x, pw):
    flat = tuple((v for pair in reversed(pw) for v in pair))
    return torch.nn.functional.pad(x, flat)
kernel_name = 'adaLN_cond__linear_kernel_hedgehog__glu_gated_geglu__prompt'

def setup_inputs():

    _w = lambda a: (lambda t: t.to(torch.bfloat16) if t.is_floating_point() else t)(torch.as_tensor(a))

    def _A_setup():
        B, S, D, H = (1, 2048, 576, 32)
        Dh = D // H
        return {'x': _w((lambda *s: torch.randn(*s, dtype=torch.bfloat16) * 0.05)(B, S, D)), 'wqkv': _w((lambda *s: torch.randn(*s, dtype=torch.bfloat16) * 0.4)(D, 3 * D)), 'w_phi': _w((lambda *s: torch.randn(*s, dtype=torch.bfloat16) * 0.4)(H, Dh, Dh) * Dh ** (-0.5)), 'wo': _w((lambda *s: torch.randn(*s, dtype=torch.bfloat16) * 0.05)(D, D))}
    _A_in = _A_setup()

    def _B_setup():
        B, S, Dm, Df = (1, 2048, 576, 4608)
        return {'x': _w((lambda *s: torch.randn(*s, dtype=torch.bfloat16) * 0.05)(B, S, Dm)), 'w_gate': _w((lambda *s: torch.randn(*s, dtype=torch.bfloat16) * 1.6)(Dm, Df) * Dm ** (-0.5)), 'w_up': _w((lambda *s: torch.randn(*s, dtype=torch.bfloat16) * 1.6)(Dm, Df) * Dm ** (-0.5)), 'w_down': _w((lambda *s: torch.randn(*s, dtype=torch.bfloat16) * 1.6)(Df, Dm) * Df ** (-0.5))}
    _B_in = _B_setup()
    return {'A_x': _A_in['x'], 'A_wqkv': _A_in['wqkv'], 'A_w_phi': _A_in['w_phi'], 'A_wo': _A_in['wo'], 'B_x': _B_in['x'], 'B_w_gate': _B_in['w_gate'], 'B_w_up': _B_in['w_up'], 'B_w_down': _B_in['w_down'], 'c': _w((lambda *s: torch.randn(*s, dtype=torch.bfloat16) * 0.1)(1, 576) * 8.0), 'w_mod': _w((lambda *s: torch.randn(*s, dtype=torch.bfloat16) * 0.1)(576, 6 * 576) * 576 ** (-0.5) * 8.0)}

def reference(A_x, A_wqkv, A_w_phi, A_wo, B_x, B_w_gate, B_w_up, B_w_down, c, w_mod):

    def _A_ref(x, wqkv, w_phi, wo):
        B, S, D = x.shape
        H = 32
        Dh = D // H
        qkv = (lambda x, *d: x.permute(*d))((x @ wqkv).reshape(B, S, 3, H, Dh), 2, 0, 3, 1, 4)
        q, k, v = (qkv[0], qkv[1], qkv[2])
        pq = torch.einsum('bhsd,hdf->bhsf', q, w_phi)
        pk = torch.einsum('bhsd,hdf->bhsf', k, w_phi)
        phi_q = torch.concatenate([_softmax(pq, axis=-1), _softmax(-pq, axis=-1)], axis=-1)
        phi_k = torch.concatenate([_softmax(pk, axis=-1), _softmax(-pk, axis=-1)], axis=-1)
        kv = torch.einsum('bhsf,bhsd->bhfd', phi_k, v)
        z = torch.einsum('bhsf->bhf', phi_k)
        num = torch.einsum('bhqf,bhfd->bhqd', phi_q, kv)
        den = torch.einsum('bhqf,bhf->bhq', phi_q, z)[..., None] + 1e-06
        o = num / den
        return (lambda x, *d: x.permute(*d))(o, 0, 2, 1, 3).reshape(B, S, D) @ wo

    def _B_ref(x, w_gate, w_up, w_down):
        g = torch.einsum('bsd,df->bsf', x, w_gate)
        u = torch.einsum('bsd,df->bsf', x, w_up)
        h = _gelu(g) * u
        return torch.einsum('bsf,fd->bsd', h, w_down)
    x = A_x
    mod = (_silu(c) @ w_mod).reshape(c.shape[0], 6, 1, -1)
    sa, ca, ga, sb, cb, gb = (mod[:, 0].clone(), mod[:, 1].clone(), mod[:, 2].clone(), mod[:, 3].clone(), mod[:, 4].clone(), mod[:, 5].clone())
    h = _layernorm(x, axis=-1) * (1 + ca) + sa
    x = x + ga * _A_ref(h, A_wqkv, A_w_phi, A_wo)
    h = _layernorm(x, axis=-1) * (1 + cb) + sb
    x = x + gb * _B_ref(h, B_w_gate, B_w_up, B_w_down)
    return x
```

```cpp
#include <hip/hip_runtime.h>
#include <hip/hip_bf16.h>
#include <stdint.h>
typedef unsigned short u16;
__device__ __forceinline__ float bf2f(u16 v){ return __uint_as_float(((unsigned)v)<<16); }
__device__ __forceinline__ u16 f2bf(float f){ unsigned u=__float_as_uint(f); u += 0x7fffu + ((u>>16)&1u); return (u16)(u>>16); }
__device__ __forceinline__ float rbf(float f){ return f; }
constexpr int S=2048, D=576, H=32, DH=18, DF=4608;

__global__ void k_mod(const u16* c, const u16* wmod, float* mod){
  int n = blockIdx.x*blockDim.x+threadIdx.x; if(n>=6*D) return;
  float acc=0;
  for(int k=0;k<D;k++){ float cv=bf2f(c[k]); float s=rbf(cv/(1.f+__expf(-cv))); acc+=s*bf2f(wmod[(size_t)k*6*D+n]); }
  mod[n]=rbf(acc);
}
__global__ void k_ln(const u16* x, const float* sh, const float* sc, u16* h){
  int row=blockIdx.x; int t=threadIdx.x;
  float v[9]; float s=0;
  for(int i=0;i<9;i++){ v[i]=bf2f(x[(size_t)row*D+t+64*i]); s+=v[i]; }
  for(int o=32;o>0;o>>=1) s+=__shfl_xor(s,o);
  float m=s/D; float q=0;
  for(int i=0;i<9;i++){ float d=v[i]-m; q+=d*d; }
  for(int o=32;o>0;o>>=1) q+=__shfl_xor(q,o);
  float r=rsqrtf(q/D+1e-5f);
  for(int i=0;i<9;i++){ int k=t+64*i; h[(size_t)row*D+k]=f2bf((v[i]-m)*r*(1.f+sc[k])+sh[k]); }
}
__global__ void k_gemm(const u16* A, const u16* B, float* C, int M,int N,int K){
  int n=blockIdx.x*blockDim.x+threadIdx.x; int m=blockIdx.y; if(n>=N) return;
  float acc=0;
  for(int k=0;k<K;k++) acc+=bf2f(A[(size_t)m*K+k])*bf2f(B[(size_t)k*N+n]);
  C[(size_t)m*N+n]=acc;
}
__global__ __launch_bounds__(256) void k_phi(const float* qkv, const u16* wphi, float* phiq, float* phik){
  int idx=blockIdx.x*blockDim.x+threadIdx.x; if(idx>=S*H*2) return;
  int which=idx/(S*H); int r=idx%(S*H); int s=r/H, h=r%H;
  float q[DH]; _Pragma("unroll") for(int d=0;d<DH;d++) q[d]=rbf(qkv[(size_t)s*3*D+which*D+h*DH+d]);
  float p[DH]; float mx=-1e30f,mn=1e30f;
  _Pragma("unroll") for(int f=0;f<DH;f++){ float a=0; _Pragma("unroll") for(int d=0;d<DH;d++) a+=q[d]*bf2f(wphi[(h*DH+d)*DH+f]); p[f]=rbf(a); mx=fmaxf(mx,p[f]); mn=fminf(mn,p[f]); }
  float s1=0,s2=0; float e1[DH],e2[DH];
  _Pragma("unroll") for(int f=0;f<DH;f++){ e1[f]=__expf(p[f]-mx); s1+=e1[f]; e2[f]=__expf(mn-p[f]); s2+=e2[f]; }
  float* o=(which?phik:phiq)+((size_t)s*H+h)*36;
  _Pragma("unroll") for(int f=0;f<DH;f++){ o[f]=rbf(e1[f]/s1); o[18+f]=rbf(e2[f]/s2); }
}
__global__ void k_kv(const float* phik, const float* qkv, float* kv){
  int idx=blockIdx.x*blockDim.x+threadIdx.x; if(idx>=H*36*19) return;
  int h=idx/(36*19); int r=idx%(36*19); int f=r/19, d=r%19;
  float acc=0;
  for(int s=0;s<S;s++){ float v = d<18 ? rbf(qkv[(size_t)s*3*D+2*D+h*DH+d]) : 1.f; acc+=phik[((size_t)s*H+h)*36+f]*v; }
  kv[idx]=rbf(acc);
}
__global__ void k_o(const float* phiq, const float* kv, u16* o){
  int idx=blockIdx.x*blockDim.x+threadIdx.x; if(idx>=S*H) return;
  int s=idx/H,h=idx%H;
  const float* p=phiq+(size_t)idx*36; const float* k=kv+h*36*19;
  float den=0; for(int f=0;f<36;f++) den+=p[f]*k[f*19+18]; den=rbf(rbf(den)+1e-6f);
  for(int d=0;d<DH;d++){ float a=0; for(int f=0;f<36;f++) a+=p[f]*k[f*19+d]; o[(size_t)s*D+h*DH+d]=f2bf(rbf(a)/den); }
}
__global__ void k_res(const u16* x, const float* y, const float* g, u16* out){
  int i=blockIdx.x*blockDim.x+threadIdx.x; if(i>=S*D) return;
  out[i]=f2bf(bf2f(x[i])+rbf(g[i%D]*rbf(y[i])));
}
__global__ void k_geglu(const float* g, const float* u, u16* h){
  size_t i=(size_t)blockIdx.x*blockDim.x+threadIdx.x; if(i>=(size_t)S*DF) return;
  float a=rbf(g[i]); float y=0.7978845608f*(a+0.044715f*a*a*a); float ge=rbf(0.5f*a*(1.f+tanhf(y)));
  h[i]=f2bf(ge*rbf(u[i]));
}
extern "C" void kernel_launch(void* const* d_in, const int* in_sizes, int n_in, void* d_out, int out_size, void* d_ws, size_t ws_size, hipStream_t stream){
  const u16* x=(const u16*)d_in[0]; const u16* wqkv=(const u16*)d_in[1]; const u16* wphi=(const u16*)d_in[2]; const u16* wo=(const u16*)d_in[3];
  const u16* wg=(const u16*)d_in[5]; const u16* wu=(const u16*)d_in[6]; const u16* wd=(const u16*)d_in[7]; const u16* c=(const u16*)d_in[8]; const u16* wmod=(const u16*)d_in[9];
  char* ws=(char*)d_ws; size_t off=0; auto al=[&](size_t b){ void* p=ws+off; off+=(b+255)&~(size_t)255; return p; };
  float* mod=(float*)al(6*D*4); u16* h=(u16*)al((size_t)S*D*2); float* qkv=(float*)al((size_t)S*3*D*4);
  float* phiq=(float*)al((size_t)S*H*36*4); float* phik=(float*)al((size_t)S*H*36*4); float* kv=(float*)al(H*36*19*4);
  u16* o=(u16*)al((size_t)S*D*2); float* t1=(float*)al((size_t)S*D*4); u16* x1=(u16*)al((size_t)S*D*2); u16* h2=(u16*)al((size_t)S*D*2);
  float* g=(float*)al((size_t)S*DF*4); float* u=(float*)al((size_t)S*DF*4); u16* hh=(u16*)al((size_t)S*DF*2);
  k_mod<<<(6*D+63)/64,64,0,stream>>>(c,wmod,mod);
  k_ln<<<S,64,0,stream>>>(x,mod,mod+D,h);
  k_gemm<<<dim3((3*D+255)/256,S),256,0,stream>>>(h,wqkv,qkv,S,3*D,D);
  k_phi<<<(S*H*2+255)/256,256,0,stream>>>(qkv,wphi,phiq,phik);
  k_kv<<<(H*36*19+63)/64,64,0,stream>>>(phik,qkv,kv);
  k_o<<<(S*H+255)/256,256,0,stream>>>(phiq,kv,o);
  k_gemm<<<dim3((D+191)/192,S),192,0,stream>>>(o,wo,t1,S,D,D);
  k_res<<<(S*D+255)/256,256,0,stream>>>(x,t1,mod+2*D,x1);
  k_ln<<<S,64,0,stream>>>(x1,mod+3*D,mod+4*D,h2);
  k_gemm<<<dim3(DF/256,S),256,0,stream>>>(h2,wg,g,S,DF,D);
  k_gemm<<<dim3(DF/256,S),256,0,stream>>>(h2,wu,u,S,DF,D);
  k_geglu<<<(S*DF)/256,256,0,stream>>>(g,u,hh);
  k_gemm<<<dim3((D+191)/192,S),192,0,stream>>>(hh,wd,t1,S,D,DF);
  k_res<<<(S*D+255)/256,256,0,stream>>>(x1,t1,mod+5*D,(u16*)d_out);
}
```

```cpp
#include <hip/hip_runtime.h>
#include <hip/hip_bf16.h>
#include <stdint.h>

typedef unsigned short u16;
typedef __attribute__((ext_vector_type(8))) short bf16x8;
typedef __attribute__((ext_vector_type(4))) short s16x4;
typedef __attribute__((ext_vector_type(4))) float f32x4;
typedef __attribute__((ext_vector_type(2))) float f32x2;
typedef __attribute__((ext_vector_type(4))) unsigned u32x4;
typedef __attribute__((ext_vector_type(2))) unsigned u32x2;
typedef __attribute__((ext_vector_type(2))) __bf16 bf16x2_t;
typedef __attribute__((address_space(3))) s16x4* lds_s16x4_p;
typedef __attribute__((address_space(1))) unsigned long long gu64;

#define DI __device__ __forceinline__
#define MFMA16(a, b, c) __builtin_amdgcn_mfma_f32_16x16x32_bf16((a), (b), (c), 0, 0, 0)
#define MFMA32(a, b, c) __builtin_amdgcn_mfma_f32_32x32x16_bf16((a), (b), (c), 0, 0, 0)
typedef __attribute__((ext_vector_type(16))) float f32x16;

constexpr int S_ = 2048, D_ = 576, DF_ = 4608;

constexpr size_t OFF_MODF = 0;
constexpr size_t OFF_KV   = 16384;
constexpr size_t SZ_KV    = 32 * 800 * 4;
constexpr size_t OFF_STAT = OFF_KV + SZ_KV;
constexpr size_t SZ_STAT  = 64 * 4 * 64 * 8;
constexpr size_t OFF_XN1  = OFF_STAT + SZ_STAT;
constexpr size_t SZ_ACT   = (size_t)128 * 18 * 1024;
constexpr size_t OFF_WQ   = OFF_XN1 + SZ_ACT;
constexpr size_t SZ_WQ    = (size_t)24 * 18 * 5 * 1024;
constexpr size_t OFF_WPHI = OFF_WQ + SZ_WQ;
constexpr size_t SZ_WPHI  = (size_t)8 * 15 * 1024;
constexpr size_t OFF_WO   = OFF_WPHI + SZ_WPHI;
constexpr size_t SZ_WO    = (size_t)36 * 18 * 1024;
constexpr size_t OFF_PHIQ = OFF_WO + SZ_WO;
constexpr size_t SZ_PHIQ  = (size_t)128 * 40 * 1024;
constexpr size_t OFF_H2   = OFF_PHIQ + SZ_PHIQ;
constexpr size_t OFF_X1   = OFF_H2 + SZ_ACT;
constexpr size_t OFF_HH   = OFF_X1 + SZ_ACT;
constexpr size_t SZ_HH    = (size_t)128 * 144 * 1024;
constexpr size_t OFF_P    = OFF_HH + SZ_HH;

#define DBG_INIT(k)
#define STAMP(i)
#define RSTAMP(i)
DI unsigned pk2(float a, float b) {
  f32x2 v = {a, b};
  bf16x2_t r = __builtin_convertvector(v, bf16x2_t);
  return __builtin_bit_cast(unsigned, r);
}
DI float lo2f(unsigned u) { return __uint_as_float(u << 16); }
DI float hi2f(unsigned u) { return __uint_as_float(u & 0xffff0000u); }
DI float bf2f(u16 v) { return __uint_as_float(((unsigned)v) << 16); }
DI float rbf(float f) { return lo2f(pk2(f, f)); }
DI float fdiv(float a, float b, float rb) {
  float q = a * rb;
  float r = __builtin_fmaf(-q, b, a);
  return __builtin_fmaf(r, rb, q);
}
DI void glds16(unsigned voff, const void* sbase, unsigned lds_dst) {
  asm volatile("s_mov_b32 m0, %2\n\ts_nop 0\n\tglobal_load_lds_dwordx4 %0, %1"
               :: "v"(voff), "s"(sbase), "s"(lds_dst));
}
DI void glds16q(unsigned voff, const void* sbase, unsigned lds_dst) {
  unsigned long long sv;
  asm volatile("s_mov_b64 %0, exec\n\ts_mov_b64 exec, 0xffff\n\ts_mov_b32 m0, %3\n\ts_nop 0\n\tglobal_load_lds_dwordx4 %1, %2\n\ts_mov_b64 exec, %0"
               : "=&s"(sv) : "v"(voff), "s"(sbase), "s"(lds_dst));
}
#define WAIT_VM(n) asm volatile("s_waitcnt vmcnt(" #n ")" ::: "memory")
#define WAIT_LGKM0() asm volatile("s_waitcnt lgkmcnt(0)" ::: "memory")
#define BARRIER() do { asm volatile("s_waitcnt lgkmcnt(0)" ::: "memory"); __builtin_amdgcn_s_barrier(); asm volatile("" ::: "memory"); } while (0)

DI bf16x8 tr_frag(unsigned a0, unsigned a1) {
  s16x4 lo = __builtin_amdgcn_ds_read_tr16_b64_v4i16((lds_s16x4_p)(size_t)a0);
  s16x4 hi = __builtin_amdgcn_ds_read_tr16_b64_v4i16((lds_s16x4_p)(size_t)a1);
  return __builtin_shufflevector(lo, hi, 0, 1, 2, 3, 4, 5, 6, 7);
}
DI const void* uniform_ptr(const void* p) {
  unsigned long long v = (unsigned long long)p;
  unsigned lo = __builtin_amdgcn_readfirstlane((unsigned)v), hi = __builtin_amdgcn_readfirstlane((unsigned)(v >> 32));
  return (const void*)(((unsigned long long)hi << 32) | lo);
}

constexpr int K0_GEMV = 108, K0_XN = 64, K0_WQ = 135, K0_WO = 41, K0_WPHI = 8, K0_ZERO = 15;
constexpr int K0_BLOCKS = K0_GEMV + K0_XN + K0_WQ + K0_WO + K0_WPHI + K0_ZERO;

__global__ __launch_bounds__(256) void k0_prep(const u16* __restrict__ x, const u16* __restrict__ wqkv, const u16* __restrict__ wphi,
                                               const u16* __restrict__ wo, const u16* __restrict__ cvec, const u16* __restrict__ wmod,
                                               char* __restrict__ ws) {
  __shared__ float red[4][32];
  const int tid = threadIdx.x, lane = tid & 63, wave = tid >> 6;
  int b = blockIdx.x;
  DBG_INIT(0);
  if (b < K0_GEMV) {
    int cgp = b;
    if (b < 96) { int grp = b >> 4, r = b & 15; cgp = 2 * (grp * 8 + (r & 7)) + (r >> 3); }
    const int gq = tid & 3, rg = tid >> 2;
    u32x4 w[9];
    float sc[9];
#pragma unroll
    for (int i = 0; i < 9; ++i) w[i] = *(const u32x4*)(wmod + (size_t)(rg + 64 * i) * 3456 + cgp * 32 + gq * 8);
#pragma unroll
    for (int i = 0; i < 9; ++i) { float cv = bf2f(cvec[rg + 64 * i]); sc[i] = rbf(cv / (1.f + __expf(-cv))); }
    float acc[8];
#pragma unroll
    for (int j = 0; j < 8; ++j) acc[j] = 0.f;
#pragma unroll
    for (int i = 0; i < 9; ++i)
#pragma unroll
      for (int j = 0; j < 4; ++j) { acc[2 * j] += sc[i] * lo2f(w[i][j]); acc[2 * j + 1] += sc[i] * hi2f(w[i][j]); }
#pragma unroll
    for (int j = 0; j < 8; ++j) {
      float v = acc[j];
      v += __shfl_xor(v, 4); v += __shfl_xor(v, 8); v += __shfl_xor(v, 16); v += __shfl_xor(v, 32);
      acc[j] = v;
    }
    if (lane < 4) {
#pragma unroll
      for (int j = 0; j < 8; ++j) red[wave][lane * 8 + j] = acc[j];
    }
    __syncthreads();
    if (tid < 32) {
      float v = rbf(red[0][tid] + red[1][tid] + red[2][tid] + red[3][tid]);
      int n = cgp * 32 + tid, part = n / 576;
      if (part == 1 || part == 4) v = rbf(1.f + v);
      ((float*)(ws + OFF_MODF))[n] = v;
    }
    STAMP(1); RSTAMP(61);
    return;
  }
  b -= K0_GEMV;
  if (b < K0_XN) {
    const int l8 = lane & 7, rr = lane >> 3, row = (b * 4 + wave) * 8 + rr;
    u32x4 v[9];
#pragma unroll
    for (int i = 0; i < 9; ++i) v[i] = *(const u32x4*)(x + (size_t)row * D_ + (8 * i + l8) * 8);
    float s = 0.f;
#pragma unroll
    for (int i = 0; i < 9; ++i)
#pragma unroll
      for (int j = 0; j < 4; ++j) s += lo2f(v[i][j]) + hi2f(v[i][j]);
    s += __shfl_xor(s, 1); s += __shfl_xor(s, 2); s += __shfl_xor(s, 4);
    const float mean = s * (1.f / 576.f);
    float q = 0.f;
#pragma unroll
    for (int i = 0; i < 9; ++i)
#pragma unroll
      for (int j = 0; j < 4; ++j) { float a = lo2f(v[i][j]) - mean, c = hi2f(v[i][j]) - mean; q += a * a + c * c; }
    q += __shfl_xor(q, 1); q += __shfl_xor(q, 2); q += __shfl_xor(q, 4);
    const float mb = rbf(mean), sd = rbf(sqrtf(rbf(rbf(q * (1.f / 576.f)) + 1e-5f))), rsd = 1.f / sd;
    u16* xn = (u16*)(ws + OFF_XN1);
    const int tt = row >> 4, t = row & 15;
#pragma unroll
    for (int i = 0; i < 9; ++i) {
      u32x4 o;
#pragma unroll
      for (int j = 0; j < 4; ++j) {
        unsigned d = pk2(lo2f(v[i][j]) - mb, hi2f(v[i][j]) - mb);
        o[j] = pk2(fdiv(lo2f(d), sd, rsd), fdiv(hi2f(d), sd, rsd));
      }
      int kg = 8 * i + l8, ks = kg >> 2, g = kg & 3;
      *(u32x4*)(xn + ((size_t)(tt * 18 + ks) * 64 + g * 16 + t) * 8) = o;
    }
    STAMP(1); RSTAMP(61);
    return;
  }
  b -= K0_XN;
  if (b < K0_WQ) {
    u16* dst = (u16*)(ws + OFF_WQ);
    u32x4 ov[4];
#pragma unroll
    for (int it = 0; it < 4; ++it) {
      int id = (b * 4 + it) * 256 + tid;
      int ln = id & 63, frag = id >> 6, tile = frag % 5, ks = (frag / 5) % 18, unit = frag / 90;
      int g = ln >> 4, m = ln & 15, slot = 4 * tile + (m & 3), hm = m >> 2;
      int col = (unit >> 3) * 576 + (4 * (unit & 7) + hm) * 18 + slot;
      const unsigned msk = slot < 18 ? 0xffffffffu : 0u;
      const u16* p = wqkv + (size_t)(32 * ks + 8 * g) * 1728 + (slot < 18 ? col : 0);
      u32x4 o;
#pragma unroll
      for (int j = 0; j < 4; ++j) o[j] = ((unsigned)p[(2 * j) * 1728] | ((unsigned)p[(2 * j + 1) * 1728] << 16)) & msk;
      ov[it] = o;
    }
#pragma unroll
    for (int it = 0; it < 4; ++it) {
      int id = (b * 4 + it) * 256 + tid;
      u32x4 o = ov[it];
      *(u32x4*)(dst + (size_t)id * 8) = o;
    }
    STAMP(1); RSTAMP(61);
    return;
  }
  b -= K0_WQ;
  if (b < K0_WO) {
    u16* dst = (u16*)(ws + OFF_WO);
    u32x4 ov[4];
#pragma unroll
    for (int it = 0; it < 4; ++it) {
      int id = min((b * 4 + it) * 256 + tid, 648 * 64 - 1);
      int ln = id & 63, frag = id >> 6, ks = frag % 18, nt = frag / 18;
      int g = ln >> 4, m = ln & 15;
#pragma unroll
      for (int j = 0; j < 4; ++j) {
        int p0 = 8 * ks + 2 * j, u0 = p0 / 18, d0 = p0 % 18;
        const u16* p = wo + (size_t)((4 * u0 + g) * 18 + d0) * 576 + nt * 16 + m;
        ov[it][j] = (unsigned)p[0] | ((unsigned)p[576] << 16);
      }
    }
#pragma unroll
    for (int it = 0; it < 4; ++it) {
      int id = (b * 4 + it) * 256 + tid;
      if (id < 648 * 64) *(u32x4*)(dst + (size_t)id * 8) = ov[it];
    }
    STAMP(1); RSTAMP(61);
    return;
  }
  b -= K0_WO;
  if (b < K0_WPHI) {
    u16* dst = (u16*)(ws + OFF_WPHI);
    u32x4 ov[4];
#pragma unroll
    for (int it = 0; it < 4; ++it) {
      int id = min((b * 4 + it) * 256 + tid, 120 * 64 - 1);
      int ln = id & 63, frag = id >> 6, ks = frag % 3, tile = (frag / 3) % 5, u = frag / 15;
      int g = ln >> 4, m = ln & 15, f = 4 * tile + (m & 3), hm = m >> 2;
#pragma unroll
      for (int j = 0; j < 4; ++j) {
        int d = 8 * ks + 2 * j;
        const bool ok = hm == g && f < 18 && d < 18;
        const u16* p = wphi + (size_t)((4 * u + g) * 18 + (ok ? d : 0)) * 18 + (ok ? f : 0);
        ov[it][j] = ((unsigned)p[0] | ((unsigned)p[18] << 16)) & (ok ? 0xffffffffu : 0u);
      }
    }
#pragma unroll
    for (int it = 0; it < 4; ++it) {
      int id = (b * 4 + it) * 256 + tid;
      if (id < 120 * 64) *(u32x4*)(dst + (size_t)id * 8) = ov[it];
    }
    STAMP(1); RSTAMP(61);
    return;
  }
  b -= K0_WPHI;
  {
    u32x4 z = {0u, 0u, 0u, 0u};
#pragma unroll
    for (int it = 0; it < 4; ++it) {
      int id = (b * 4 + it) * 256 + tid;
      if (id < (int)((SZ_KV + SZ_STAT) / 16)) *(u32x4*)(ws + OFF_KV + (size_t)id * 16) = z;
    }
    STAMP(1); RSTAMP(61);
  }
}

constexpr int K1_CH = 6 * 10 * 1024;
constexpr int K1_MOD = 2 * K1_CH;
constexpr int K1_PHI = K1_MOD + 4608;
constexpr int K1_LDS = K1_PHI + 30720;
constexpr int PHI_STR = 352, V_STR = 224, V_OFF = 128 * PHI_STR;

DI void feature_map(const f32x4 (&acc)[5], const char* wphi_lds, u32x4 (&phi)[5]) {
  bf16x8 qf[3];
  {
    u32x4 a0 = {pk2(acc[0][0], acc[0][1]), pk2(acc[0][2], acc[0][3]), pk2(acc[1][0], acc[1][1]), pk2(acc[1][2], acc[1][3])};
    u32x4 a1 = {pk2(acc[2][0], acc[2][1]), pk2(acc[2][2], acc[2][3]), pk2(acc[3][0], acc[3][1]), pk2(acc[3][2], acc[3][3])};
    u32x4 a2 = {pk2(acc[4][0], acc[4][1]), 0u, 0u, 0u};
    qf[0] = __builtin_bit_cast(bf16x8, a0); qf[1] = __builtin_bit_cast(bf16x8, a1); qf[2] = __builtin_bit_cast(bf16x8, a2);
  }
  f32x4 c[5];
#pragma unroll
  for (int i = 0; i < 5; ++i) c[i] = (f32x4){0.f, 0.f, 0.f, 0.f};
#pragma unroll
  for (int ks = 0; ks < 3; ++ks)
#pragma unroll
    for (int i = 0; i < 5; ++i) {
      bf16x8 w = *(const bf16x8*)(wphi_lds + (i * 3 + ks) * 1024);
      c[i] = MFMA16(w, qf[ks], c[i]);
    }
  float p[20];
#pragma unroll
  for (int i = 0; i < 5; ++i)
#pragma unroll
    for (int r = 0; r < 4; r += 2) { unsigned u = pk2(c[i][r], c[i][r + 1]); p[4 * i + r] = lo2f(u); p[4 * i + r + 1] = hi2f(u); }
  float mx = fmaxf(p[0], p[1]), mn = fminf(p[0], p[1]);
#pragma unroll
  for (int f = 2; f < 18; f += 2) { mx = __builtin_fmaxf(__builtin_fmaxf(mx, p[f]), p[f + 1]); mn = __builtin_fminf(__builtin_fminf(mn, p[f]), p[f + 1]); }
  const float L2E = 1.4426950408889634f;
  const float mxs = -mx * L2E, mns = mn * L2E;
  float e1[18], e2[18], s1 = 0.f, s2 = 0.f;
#pragma unroll
  for (int f = 0; f < 18; ++f) {
    e1[f] = __builtin_amdgcn_exp2f(__builtin_fmaf(p[f], L2E, mxs)); s1 += e1[f];
    e2[f] = __builtin_amdgcn_exp2f(__builtin_fmaf(p[f], -L2E, mns)); s2 += e2[f];
  }
  const float r1 = __builtin_amdgcn_rcpf(s1), r2 = __builtin_amdgcn_rcpf(s2);
  unsigned o[20];
#pragma unroll
  for (int f = 0; f < 18; f += 2) {
    o[f >> 1] = pk2(e1[f] * r1, e1[f + 1] * r1);
    o[9 + (f >> 1)] = pk2(e2[f] * r2, e2[f + 1] * r2);
  }
  o[18] = 0u; o[19] = 0u;
#pragma unroll
  for (int i = 0; i < 5; ++i) { phi[i][0] = o[4 * i]; phi[i][1] = o[4 * i + 1]; phi[i][2] = o[4 * i + 2]; phi[i][3] = o[4 * i + 3]; }
}

__global__ __launch_bounds__(512, 2) void k1_qkv(char* __restrict__ ws, const u16* __restrict__ wg, const u16* __restrict__ wu, const u16* __restrict__ wd) {
  extern __shared__ __attribute__((aligned(16))) char smem[];
  const int tid = threadIdx.x, lane = tid & 63;
  const int wave = __builtin_amdgcn_readfirstlane(tid >> 6);
  const int b = blockIdx.x, xcd = b & 7, idx = b >> 3;
  if (b >= 192) {
    const int tb = b - 192;
    u32x4 a = {0u, 0u, 0u, 0u};
#pragma unroll 1
    for (int m = 0; m < 3; ++m) {
      const u32x4* p = (const u32x4*)(m == 0 ? wd : (m == 1 ? wg : wu)) + (size_t)tb * 5184 + tid;
      u32x4 v[10];
#pragma unroll
      for (int i = 0; i < 10; ++i) v[i] = p[i * 512];
      u32x4 v10 = p[tid < 64 ? 5120 : 0];
#pragma unroll
      for (int i = 0; i < 10; ++i) a ^= v[i];
      a ^= v10;
    }
    asm volatile("" :: "v"(a));
    return;
  }
  const int mt = xcd * 2 + idx / 12, cg = idx % 12;
  const bool isq = cg < 4;
  const int uA = isq ? 2 * cg : 8 + (cg - 4), uB = isq ? 2 * cg + 1 : 16 + (cg - 4);
  const int tt = mt * 8 + wave, g = lane >> 4, t = lane & 15;
  const unsigned lds0 = (unsigned)(size_t)smem;
  const unsigned vo = (unsigned)lane * 16u;
  DBG_INIT(1);
  const char* wsrc = ws + OFF_WQ + (size_t)((wave < 4 ? uA : uB) * 18) * 5120 + (wave & 3) * 7680;
  const unsigned wdst = lds0 + (wave >> 2) * 30720 + (wave & 3) * 7680;
  auto issue_chunk = [&](int c) __attribute__((always_inline)) {
    const char* sp = wsrc + (size_t)c * 30720;
    const unsigned dp = wdst + (c & 1) * K1_CH;
#pragma unroll
    for (int i = 0; i < 7; ++i) glds16(vo, sp + i * 1024, dp + i * 1024);
    glds16q(vo, sp + 7168, dp + 7168);
    glds16q(vo, sp + 7424, dp + 7424);
  };
  u32x4 xn[18];
  const u16* xp = (const u16*)(ws + OFF_XN1) + ((size_t)tt * 18 * 64 + lane) * 8;
  {
    f32x4 mv;
    const float* mf = (const float*)(ws + OFF_MODF);
    if (tid < 288) mv = *(const f32x4*)(mf + (tid < 144 ? 576 + tid * 4 : (tid - 144) * 4));
#pragma unroll
    for (int ks = 0; ks < 7; ++ks) xn[ks] = *(const u32x4*)(xp + (size_t)ks * 512);
    issue_chunk(0);
    if (tid < 288) *(f32x4*)(smem + K1_MOD + tid * 16) = mv;
  }
  STAMP(1);
  WAIT_VM(0);
#pragma unroll
  for (int ks = 0; ks < 7; ++ks) asm volatile("" : "+v"(xn[ks]));
  STAMP(2);
  BARRIER();
  STAMP(3);
  const char* ps = ws + OFF_WPHI + (size_t)(uA & 7) * 15360 + wave * 4096;
  const unsigned pd = lds0 + K1_PHI + wave * 4096;
  const int npp = isq ? (wave < 7 ? 4 : 2) : (wave < 3 ? 4 : (wave == 3 ? 3 : 0));
  f32x4 acc[2][5];
#pragma unroll
  for (int u = 0; u < 2; ++u)
#pragma unroll
    for (int i = 0; i < 5; ++i) acc[u][i] = (f32x4){0.f, 0.f, 0.f, 0.f};
  const float* modl = (const float*)(smem + K1_MOD) + g * 8;
  auto modulate = [&](int ks) __attribute__((always_inline)) -> bf16x8 {
    const f32x4 c1a = *(const f32x4*)(modl + ks * 32), c1b = *(const f32x4*)(modl + ks * 32 + 4);
    const f32x4 saa = *(const f32x4*)(modl + 576 + ks * 32), sab = *(const f32x4*)(modl + 576 + ks * 32 + 4);
    u32x4 hq;
#pragma unroll
    for (int j = 0; j < 4; ++j) {
      float c0 = j < 2 ? c1a[2 * j] : c1b[2 * j - 4], c1 = j < 2 ? c1a[2 * j + 1] : c1b[2 * j - 3];
      float s0 = j < 2 ? saa[2 * j] : sab[2 * j - 4], s1 = j < 2 ? saa[2 * j + 1] : sab[2 * j - 3];
      unsigned q = pk2(lo2f(xn[ks][j]) * c0, hi2f(xn[ks][j]) * c1);
      hq[j] = pk2(lo2f(q) + s0, hi2f(q) + s1);
    }
    return __builtin_bit_cast(bf16x8, hq);
  };
  bf16x8 w[2][10], hf[2];
  auto loadw = [&](int ks, bf16x8 (&wr)[10]) __attribute__((always_inline)) {
    const char* wb = smem + ((ks / 6) & 1) * K1_CH + (ks % 6) * 5120 + lane * 16;
#pragma unroll
    for (int u = 0; u < 2; ++u)
#pragma unroll
      for (int i = 0; i < 5; ++i) wr[u * 5 + i] = *(const bf16x8*)(wb + u * 30720 + i * 1024);
  };
  hf[0] = modulate(0);
  loadw(0, w[0]);
#pragma unroll
  for (int ks = 0; ks < 18; ++ks) {
    if (ks == 5 || ks == 11) { STAMP(ks); WAIT_VM(0); STAMP(ks+1); BARRIER(); STAMP(ks+2); if (ks == 5) issue_chunk(2); STAMP(ks+3); }
    if (ks == 0) issue_chunk(1);
    if (ks >= 1 && ks <= 3) {
#pragma unroll
      for (int q = 0; q < 4; ++q) if (7 + (ks - 1) * 4 + q < 18) xn[7 + (ks - 1) * 4 + q] = *(const u32x4*)(xp + (size_t)(7 + (ks - 1) * 4 + q) * 512);
    }
    if (ks == 4) {
      for (int i = 0; i < npp; ++i) glds16(vo, ps + i * 1024, pd + i * 1024);
    }
    if (ks < 17) { hf[(ks + 1) & 1] = modulate(ks + 1); loadw(ks + 1, w[(ks + 1) & 1]); }
#pragma unroll
    for (int u = 0; u < 2; ++u)
#pragma unroll
      for (int i = 0; i < 5; ++i) acc[u][i] = MFMA16(w[ks & 1][u * 5 + i], hf[ks & 1], acc[u][i]);
    if (ks < 17) {
      __builtin_amdgcn_sched_group_barrier(0x100, 4, 0);
#pragma unroll
      for (int i = 0; i < 10; ++i) {
        __builtin_amdgcn_sched_group_barrier(0x8, 1, 0);
        __builtin_amdgcn_sched_group_barrier(0x100, 1, 0);
        __builtin_amdgcn_sched_group_barrier(0x2, 3, 0);
      }
    }
    __builtin_amdgcn_sched_barrier(0);
  }
  STAMP(15);
  if (isq) {
    u16* pq = (u16*)(ws + OFF_PHIQ);
#pragma unroll
    for (int u = 0; u < 2; ++u) {
      u32x4 phi[5];
      feature_map(acc[u], smem + K1_PHI + u * 15360 + lane * 16, phi);
#pragma unroll
      for (int i = 0; i < 5; ++i) *(u32x4*)(pq + (((size_t)(tt * 8 + uA + u) * 5 + i) * 64 + lane) * 8) = phi[i];
      STAMP(16+u);
    }
    RSTAMP(61);
    return;
  }
  u32x4 phi[5];
  feature_map(acc[0], smem + K1_PHI + lane * 16, phi);
#pragma unroll
  for (int i = 0; i < 5; ++i) asm volatile("" : "+v"(phi[i]));
  STAMP(16);
  BARRIER();
  STAMP(17);
  {
    const int tk = wave * 16 + t;
    char* pr = smem + tk * PHI_STR + g * 80;
#pragma unroll
    for (int i = 0; i < 5; ++i) *(u32x4*)(pr + i * 16) = phi[i];
    char* vr = smem + V_OFF + tk * V_STR + g * 40;
#pragma unroll
    for (int i = 0; i < 4; ++i) *(u32x2*)(vr + i * 8) = (u32x2){pk2(acc[1][i][0], acc[1][i][1]), pk2(acc[1][i][2], acc[1][i][3])};
    *(u32x2*)(vr + 32) = (u32x2){pk2(acc[1][4][0], acc[1][4][1]), pk2(1.f, 0.f)};
  }
  BARRIER();
  STAMP(18);
  {
    const int hd = wave >> 1, mtile = wave & 1, q4 = (lane & 15) >> 2, p = lane & 3;
    f32x4 kacc[3];
#pragma unroll
    for (int n = 0; n < 3; ++n) kacc[n] = (f32x4){0.f, 0.f, 0.f, 0.f};
    const unsigned va = lds0 + V_OFF + (4 * g + q4) * V_STR + hd * 40 + mtile * 32 + p * 8;
    const unsigned pa = lds0 + (4 * g + q4) * PHI_STR + hd * 80 + p * 8;
#pragma unroll
    for (int ks = 0; ks < 4; ++ks) {
      bf16x8 a = tr_frag(va + (32 * ks) * V_STR, va + (32 * ks + 16) * V_STR);
#pragma unroll
      for (int n = 0; n < 3; ++n) {
        bf16x8 bb = tr_frag(pa + (32 * ks) * PHI_STR + n * 32, pa + (32 * ks + 16) * PHI_STR + n * 32);
        kacc[n] = MFMA16(a, bb, kacc[n]);
      }
    }
    STAMP(19);
    float* kv = (float*)(ws + OFF_KV) + (size_t)(4 * (uA - 8) + hd) * 800 + (mtile * 16 + 4 * g) * 40 + t;
    if (mtile == 0) {
#pragma unroll
      for (int r = 0; r < 4; ++r) { atomicAdd(kv + r * 40, kacc[0][r]); atomicAdd(kv + r * 40 + 16, kacc[1][r]); }
      if (t < 4) {
#pragma unroll
        for (int r = 0; r < 4; ++r) atomicAdd(kv + r * 40 + 32, kacc[2][r]);
      }
    } else if (g == 0) {
#pragma unroll
      for (int r = 0; r < 3; ++r) { atomicAdd(kv + r * 40, kacc[0][r]); atomicAdd(kv + r * 40 + 16, kacc[1][r]); }
      if (t < 4) {
#pragma unroll
        for (int r = 0; r < 3; ++r) atomicAdd(kv + r * 40 + 32, kacc[2][r]);
      }
    }
    STAMP(20);
    STAMP(21); RSTAMP(61);
  }
}

constexpr int K2_KV = 36864, K2_ZERO = K2_KV + 51200, K2_ST = K2_ZERO + 1600, K2_LDS = K2_ST + 9 * 32 * 8 + 32 * 8;

__global__ __launch_bounds__(576) void k2_attn(const u16* __restrict__ x, char* __restrict__ ws) {
  extern __shared__ __attribute__((aligned(16))) char smem[];
  const int tid = threadIdx.x, lane = tid & 63;
  const int wave = __builtin_amdgcn_readfirstlane(tid >> 6);
  const int b = blockIdx.x, xcd = b & 7, idx = b >> 3;
  const int rt = xcd * 8 + (idx >> 2), cb = idx & 3;
  const int g = lane >> 4, t = lane & 15;
  const int nt = cb * 9 + wave;
  DBG_INIT(2);
  {
    const f32x4* kvp = (const f32x4*)(ws + OFF_KV);
    f32x4 kvv[12];
#pragma unroll
    for (int i = 0; i < 11; ++i) kvv[i] = kvp[i * 576 + tid];
    kvv[11] = kvp[tid < 64 ? 6336 + tid : tid];
#pragma unroll
    for (int i = 0; i < 11; ++i) *(u32x2*)(smem + K2_KV + (i * 576 + tid) * 8) = (u32x2){pk2(kvv[i][0], kvv[i][1]), pk2(kvv[i][2], kvv[i][3])};
    if (tid < 64) *(u32x2*)(smem + K2_KV + (6336 + tid) * 8) = (u32x2){pk2(kvv[11][0], kvv[11][1]), pk2(kvv[11][2], kvv[11][3])};
    else if (tid < 164) *(u32x4*)(smem + K2_ZERO + (tid - 64) * 16) = (u32x4){0u, 0u, 0u, 0u};
  }
  bf16x8 ph[2][5];
  {
    const int u = wave & 7;
    const u16* pq = (const u16*)(ws + OFF_PHIQ);
#pragma unroll
    for (int a = 0; a < 2; ++a)
#pragma unroll
      for (int s = 0; s < 5; ++s) ph[a][s] = *(const bf16x8*)(pq + (((size_t)((rt * 2 + a) * 8 + u) * 5 + s) * 64 + lane) * 8);
  }
  bf16x8 wf[18];
  const u16* wp = (const u16*)(ws + OFF_WO) + ((size_t)nt * 18 * 64 + lane) * 8;
#pragma unroll
  for (int ks = 0; ks < 9; ++ks) wf[ks] = *(const bf16x8*)(wp + (size_t)ks * 512);
  STAMP(1);
  __syncthreads();
  STAMP(2);
  if (wave < 8) {
    const int u = wave;
    f32x4 oacc[2][5];
#pragma unroll
    for (int a = 0; a < 2; ++a)
#pragma unroll
      for (int i = 0; i < 5; ++i) oacc[a][i] = (f32x4){0.f, 0.f, 0.f, 0.f};
    const char* kb = smem + (((t >> 2) == g) ? K2_KV + (4 * u + g) * 1600 + (t & 3) * 80 : K2_ZERO);
#pragma unroll
    for (int s = 0; s < 5; ++s) {
#pragma unroll
      for (int i = 0; i < 5; ++i) {
        const bf16x8 a8 = *(const bf16x8*)(kb + i * 320 + s * 16);
        oacc[0][i] = MFMA16(a8, ph[0][s], oacc[0][i]);
        oacc[1][i] = MFMA16(a8, ph[1][s], oacc[1][i]);
      }
    }
#pragma unroll
    for (int a = 0; a < 2; ++a) {
      const float den = rbf(rbf(oacc[a][4][2]) + 1e-6f), rden = __builtin_amdgcn_rcpf(den);
#pragma unroll
      for (int d = 0; d < 18; d += 2) {
        unsigned nq = pk2(oacc[a][d >> 2][d & 3], oacc[a][d >> 2][(d & 3) + 1]);
        unsigned o = pk2(fdiv(lo2f(nq), den, rden), fdiv(hi2f(nq), den, rden));
        const int pos = 18 * u + d;
        *(unsigned*)(smem + ((a * 18 + (pos >> 3)) * 64 + lane) * 16 + (pos & 7) * 2) = o;
      }
    }
  }
  asm volatile("" ::: "memory");
#pragma unroll
  for (int ks = 9; ks < 18; ++ks) wf[ks] = *(const bf16x8*)(wp + (size_t)ks * 512);
  const float* mf = (const float*)(ws + OFF_MODF);
  const int n0 = nt * 16 + 4 * g;
  const f32x4 ga = *(const f32x4*)(mf + 2 * 576 + n0), sb = *(const f32x4*)(mf + 3 * 576 + n0), c1b = *(const f32x4*)(mf + 4 * 576 + n0);
  u32x2 xv[2];
#pragma unroll
  for (int a = 0; a < 2; ++a) xv[a] = *(const u32x2*)(x + (size_t)((rt * 2 + a) * 16 + t) * D_ + n0);
  STAMP(3);
  __syncthreads();
  STAMP(4);
  f32x4 acc[2] = {{0.f, 0.f, 0.f, 0.f}, {0.f, 0.f, 0.f, 0.f}};
#pragma unroll
  for (int ks = 0; ks < 18; ++ks)
#pragma unroll
    for (int a = 0; a < 2; ++a) {
      bf16x8 of = *(const bf16x8*)(smem + ((a * 18 + ks) * 64 + lane) * 16);
      acc[a] = MFMA16(wf[ks], of, acc[a]);
    }
  float x1[2][4];
  float* st = (float*)(smem + K2_ST);
  float* fin = st + 9 * 32 * 2;
#pragma unroll
  for (int a = 0; a < 2; ++a) {
    unsigned a01 = pk2(acc[a][0], acc[a][1]), a23 = pk2(acc[a][2], acc[a][3]);
    unsigned y01 = pk2(ga[0] * lo2f(a01), ga[1] * hi2f(a01)), y23 = pk2(ga[2] * lo2f(a23), ga[3] * hi2f(a23));
    unsigned z01 = pk2(lo2f(xv[a][0]) + lo2f(y01), hi2f(xv[a][0]) + hi2f(y01));
    unsigned z23 = pk2(lo2f(xv[a][1]) + lo2f(y23), hi2f(xv[a][1]) + hi2f(y23));
    x1[a][0] = lo2f(z01); x1[a][1] = hi2f(z01); x1[a][2] = lo2f(z23); x1[a][3] = hi2f(z23);
    *(u32x2*)((u16*)(ws + OFF_X1) + (((size_t)(rt * 2 + a) * 36 + nt) * 64 + lane) * 4) = (u32x2){z01, z23};
    float s = (x1[a][0] + x1[a][1]) + (x1[a][2] + x1[a][3]);
    s += __shfl_xor(s, 16); s += __shfl_xor(s, 32);
    const float mw = s * (1.f / 16.f);
    float q = 0.f;
#pragma unroll
    for (int r = 0; r < 4; ++r) { float d = x1[a][r] - mw; q += d * d; }
    q += __shfl_xor(q, 16); q += __shfl_xor(q, 32);
    if (g == 0) *(f32x2*)(st + (wave * 32 + a * 16 + t) * 2) = (f32x2){mw, q};
  }
  STAMP(5);
  __syncthreads();
  STAMP(6);
  if (wave == 0) {
    const int tok = lane & 31;
    float mw[9], s = 0.f, q = 0.f;
#pragma unroll
    for (int w = 0; w < 9; ++w) { f32x2 v = *(const f32x2*)(st + (w * 32 + tok) * 2); mw[w] = v[0]; s += v[0]; q += v[1]; }
    const float mb = s * (1.f / 9.f);
    float dq = 0.f;
#pragma unroll
    for (int w = 0; w < 9; ++w) { float d = mw[w] - mb; dq += d * d; }
    q += 16.f * dq;
    const float mine = lane < 32 ? mb : q;
    gu64* G = (gu64*)(ws + OFF_STAT) + (size_t)rt * 4 * 64;
    __hip_atomic_store(G + cb * 64 + lane, (1ull << 32) | (unsigned long long)__float_as_uint(mine), __ATOMIC_RELAXED, __HIP_MEMORY_SCOPE_AGENT);
    float vals[4];
    for (unsigned spins = 0; spins < (1u << 22); ++spins) {
      bool ok = true;
#pragma unroll
      for (int c = 0; c < 4; ++c) {
        unsigned long long v = __hip_atomic_load(G + c * 64 + lane, __ATOMIC_RELAXED, __HIP_MEMORY_SCOPE_AGENT);
        vals[c] = __uint_as_float((unsigned)v);
        ok &= (v >> 32) == 1ull;
      }
      if (__all(ok)) break;
      __builtin_amdgcn_s_sleep(1);
    }
    float m2 = (vals[0] + vals[1]) + (vals[2] + vals[3]);
    const float mean = __shfl(m2, tok) * 0.25f;
    float dd = 0.f;
#pragma unroll
    for (int c = 0; c < 4; ++c) { float d = vals[c] - mean; dd += d * d; }
    const float M2 = __shfl(m2, 32 + tok) + 144.f * dd;
    if (lane < 32) {
      const float mbf = rbf(mean), sd = rbf(sqrtf(rbf(rbf(M2 * (1.f / 576.f)) + 1e-5f)));
      *(f32x2*)(fin + lane * 2) = (f32x2){mbf, sd};
    }
  }
  STAMP(7);
  __syncthreads();
  STAMP(8);
#pragma unroll
  for (int a = 0; a < 2; ++a) {
    const f32x2 ms = *(const f32x2*)(fin + (a * 16 + t) * 2);
    const float rsd = 1.f / ms[1];
    unsigned d01 = pk2(x1[a][0] - ms[0], x1[a][1] - ms[0]), d23 = pk2(x1[a][2] - ms[0], x1[a][3] - ms[0]);
    unsigned n01 = pk2(fdiv(lo2f(d01), ms[1], rsd), fdiv(hi2f(d01), ms[1], rsd));
    unsigned n23 = pk2(fdiv(lo2f(d23), ms[1], rsd), fdiv(hi2f(d23), ms[1], rsd));
    unsigned m01 = pk2(lo2f(n01) * c1b[0], hi2f(n01) * c1b[1]), m23 = pk2(lo2f(n23) * c1b[2], hi2f(n23) * c1b[3]);
    unsigned h01 = pk2(lo2f(m01) + sb[0], hi2f(m01) + sb[1]), h23 = pk2(lo2f(m23) + sb[2], hi2f(m23) + sb[3]);
    *(u32x2*)((u16*)(ws + OFF_H2) + (((size_t)rt * 36 + nt) * 64 + (g & 1) * 32 + a * 16 + t) * 8 + (g >> 1) * 4) = (u32x2){h01, h23};
  }
  STAMP(9); RSTAMP(61);
}

constexpr int K3_UOFF = 9216 + 128, K3_AOFF = 18688, K3_SLOT = K3_AOFF + 16384;

DI float geglu(float gv, float uv) {
  const float k0 = -2.302208198f;
  const float k1 = -0.1029432397f;
  float tq = gv * gv;
  float pz = __builtin_fmaf(tq, k1, k0);
  float e = __builtin_amdgcn_exp2f(gv * pz);
  float r = __builtin_amdgcn_rcpf(1.f + e);
  return gv * r * uv;
}

__global__ __launch_bounds__(512, 2) void k3_ffn1(const u16* __restrict__ wg, const u16* __restrict__ wu, char* __restrict__ ws) {
  extern __shared__ __attribute__((aligned(16))) char smem[];
  const int tid = threadIdx.x, lane = tid & 63;
  const int wave = __builtin_amdgcn_readfirstlane(tid >> 6);
  const int b = blockIdx.x, xcd = b & 7, idx = b >> 3;
  const int ntile = xcd * 4 + (idx & 3), mtile = idx >> 2;
  const unsigned lds0 = (unsigned)(size_t)smem;
  const int T = mtile * 8 + wave;
  const int gl0 = wave * 64 + lane, gl1 = (wave == 0 ? 512 : (wave - 1) * 64) + lane, gl2 = 448 + wave * 16 + (lane & 15);
  const unsigned vo0 = (unsigned)((gl0 / 18) * (DF_ * 2) + (gl0 % 18) * 16);
  const unsigned vo1 = (unsigned)((gl1 / 18) * (DF_ * 2) + (gl1 % 18) * 16);
  const unsigned vo2 = (unsigned)((gl2 / 18) * (DF_ * 2) + (gl2 % 18) * 16);
  const unsigned voa = (unsigned)lane * 16u;
  const char* sg = (const char*)wg + ntile * 288;
  const char* su = (const char*)wu + ntile * 288;
  const char* s1 = wave == 0 ? sg : su;
  const char* sa = ws + OFF_H2 + (size_t)T * 36 * 1024;
  const unsigned d0 = lds0 + wave * 1024, d1 = lds0 + (wave == 0 ? 8192 : K3_UOFF + (wave - 1) * 1024);
  const unsigned d2 = lds0 + K3_UOFF + 7168 + wave * 256, d3 = lds0 + K3_AOFF + wave * 2048;
  auto piece = [&](int st, int i) {
    const size_t dl = (size_t)st * 32 * (DF_ * 2);
    const unsigned sl = (st & 3) * K3_SLOT;
    if (i == 0) glds16(vo0, sg + dl, d0 + sl);
    else if (i == 1) glds16(vo1, s1 + dl, d1 + sl);
    else if (i == 2) glds16q(vo2, su + dl, d2 + sl);
    else if (i == 3) glds16(voa, sa + (size_t)st * 2048, d3 + sl);
    else glds16(voa, sa + (size_t)st * 2048 + 1024, d3 + sl + 1024);
  };
#pragma unroll
  for (int st = 0; st < 3; ++st)
#pragma unroll
    for (int i = 0; i < 5; ++i) piece(st, i);
  f32x16 acc[9];
#pragma unroll
  for (int i = 0; i < 9; ++i)
#pragma unroll
    for (int r = 0; r < 16; ++r) acc[i][r] = 0.f;
  const int h = lane >> 5, grp = (lane >> 4) & 1, q4 = (lane & 15) >> 2, p = lane & 3;
  const unsigned rbase = lds0 + (grp ? K3_UOFF : 0) + (4 * h + q4) * 288 + p * 8;
  const unsigned abase = K3_AOFF + wave * 2048 + lane * 16;
  bf16x8 wA[9], wB[9], aA, aB;
#pragma unroll
  for (int i = 0; i < 9; ++i) wB[i] = (bf16x8){0, 0, 0, 0, 0, 0, 0, 0};
  aB = (bf16x8){0, 0, 0, 0, 0, 0, 0, 0};
  auto body = [&](int st, bool pf) __attribute__((always_inline)) {
    BARRIER();
    const unsigned sl = (st & 3) * K3_SLOT;
    aA = *(const bf16x8*)(smem + sl + abase);
#pragma unroll
    for (int tn = 0; tn < 9; ++tn) {
      const unsigned ad = rbase + sl + tn * 32;
      wA[tn] = tr_frag(ad, ad + 8 * 288);
      acc[tn] = MFMA32(wB[tn], aB, acc[tn]);
      if (pf && (tn == 1 || tn == 4 || tn == 7)) piece(st + 3, tn / 3);
    }
    __builtin_amdgcn_sched_group_barrier(0x100, 3, 0);
#pragma unroll
    for (int tn = 0; tn < 8; ++tn) { __builtin_amdgcn_sched_group_barrier(0x8, 1, 0); __builtin_amdgcn_sched_group_barrier(0x100, 2, 0); }
    __builtin_amdgcn_sched_group_barrier(0x8, 1, 0);
    __builtin_amdgcn_sched_barrier(0);
    aB = *(const bf16x8*)(smem + sl + abase + 1024);
#pragma unroll
    for (int tn = 0; tn < 9; ++tn) {
      const unsigned ad = rbase + sl + 16 * 288 + tn * 32;
      wB[tn] = tr_frag(ad, ad + 8 * 288);
      acc[tn] = MFMA32(wA[tn], aA, acc[tn]);
      if (pf && (tn == 1 || tn == 4)) piece(st + 3, 3 + tn / 3);
    }
    __builtin_amdgcn_sched_group_barrier(0x100, 3, 1);
#pragma unroll
    for (int tn = 0; tn < 8; ++tn) { __builtin_amdgcn_sched_group_barrier(0x8, 1, 1); __builtin_amdgcn_sched_group_barrier(0x100, 2, 1); }
    __builtin_amdgcn_sched_group_barrier(0x8, 1, 1);
    __builtin_amdgcn_sched_barrier(0);
  };
#pragma unroll 1
  for (int st = 0; st < 15; ++st) { WAIT_VM(10); body(st, true); }
  WAIT_VM(10); body(15, false);
  WAIT_VM(5); body(16, false);
  WAIT_VM(0); body(17, false);
#pragma unroll
  for (int tn = 0; tn < 9; ++tn) acc[tn] = MFMA32(wB[tn], aB, acc[tn]);
  u16* hh = (u16*)(ws + OFF_HH);
  const int t32 = lane & 31;
#pragma unroll
  for (int tn = 0; tn < 9; ++tn) {
    u32x4 o;
#pragma unroll
    for (int j = 0; j < 4; ++j) o[j] = pk2(geglu(acc[tn][2 * j], acc[tn][8 + 2 * j]), geglu(acc[tn][2 * j + 1], acc[tn][9 + 2 * j]));
    const int ft = ntile * 9 + tn;
    *(u32x4*)(hh + (((size_t)(2 * T + (t32 >> 4)) * 144 + (ft >> 1)) * 64 + (2 * (ft & 1) + h) * 16 + (t32 & 15)) * 8) = o;
  }
}

constexpr int K4_SLOT = 18432 + 16384;

__global__ __launch_bounds__(512, 2) void k4_ffn2(const u16* __restrict__ wd, char* __restrict__ ws) {
  extern __shared__ __attribute__((aligned(16))) char smem[];
  const int tid = threadIdx.x, lane = tid & 63;
  const int wave = __builtin_amdgcn_readfirstlane(tid >> 6);
  const int b = blockIdx.x, xcd = b & 7, idx = b >> 3;
  const int sp = xcd >> 1, mtile = (xcd & 1) * 8 + (idx >> 2), nb = idx & 3;
  const int tg = wave & 3, kh = wave >> 2;
  const int g = lane >> 4, q4 = (lane & 15) >> 2, p = lane & 3;
  const unsigned lds0 = (unsigned)(size_t)smem;
  const int tt0 = mtile * 8 + tg * 2;
  const int gl0 = tid, gl1 = 512 + tid, gl2 = 1024 + wave * 16 + (lane & 15);
  const unsigned vo0 = (unsigned)((gl0 / 18) * (D_ * 2) + (gl0 % 18) * 16);
  const unsigned vo1 = (unsigned)((gl1 / 18) * (D_ * 2) + (gl1 % 18) * 16);
  const unsigned vo2 = (unsigned)((gl2 / 18) * (D_ * 2) + (gl2 % 18) * 16);
  const unsigned voa = (unsigned)lane * 16u;
  const char* sw = (const char*)wd + (size_t)(1152 * sp) * (D_ * 2) + nb * 288;
  const char* sh = ws + OFF_HH + ((size_t)tt0 * 144 + 36 * sp + kh) * 1024;
  const unsigned d0 = lds0 + wave * 1024, d2 = lds0 + 16384 + wave * 256, d3 = lds0 + 18432 + wave * 2048;
  auto piece = [&](int st, int i) {
    const size_t dl = (size_t)st * 64 * (D_ * 2);
    const unsigned sl = (st & 3) * K4_SLOT;
    if (i == 0) glds16(vo0, sw + dl, d0 + sl);
    else if (i == 1) glds16(vo1, sw + dl, d0 + sl + 8192);
    else if (i == 2) glds16q(vo2, sw + dl, d2 + sl);
    else if (i == 3) glds16(voa, sh + (size_t)st * 2048, d3 + sl);
    else glds16(voa, sh + (size_t)st * 2048 + 144 * 1024, d3 + sl + 1024);
  };
#pragma unroll
  for (int st = 0; st < 3; ++st)
#pragma unroll
    for (int i = 0; i < 5; ++i) piece(st, i);
  f32x4 acc[2][9];
#pragma unroll
  for (int a = 0; a < 2; ++a)
#pragma unroll
    for (int i = 0; i < 9; ++i) acc[a][i] = (f32x4){0.f, 0.f, 0.f, 0.f};
  const unsigned rbase = lds0 + (32 * kh + 16 * (g >> 1) + 4 * (g & 1) + q4) * 288 + p * 8;
  const unsigned abase = 18432 + wave * 2048 + lane * 16;
  bf16x8 wA[9], wB[9], hA0, hA1, hB0, hB1;
#pragma unroll
  for (int i = 0; i < 9; ++i) wB[i] = (bf16x8){0, 0, 0, 0, 0, 0, 0, 0};
  hB0 = (bf16x8){0, 0, 0, 0, 0, 0, 0, 0}; hB1 = hB0;
  auto body = [&](int st, bool pf, bf16x8 (&wN)[9], bf16x8& hN0, bf16x8& hN1, bf16x8 (&wO)[9], bf16x8& hO0, bf16x8& hO1) __attribute__((always_inline)) {
    BARRIER();
    const unsigned sl = (st & 3) * K4_SLOT;
    hN0 = *(const bf16x8*)(smem + sl + abase);
    hN1 = *(const bf16x8*)(smem + sl + abase + 1024);
#pragma unroll
    for (int i = 0; i < 9; ++i) {
      const unsigned ad = rbase + sl + i * 32;
      wN[i] = tr_frag(ad, ad + 8 * 288);
      acc[0][i] = MFMA16(wO[i], hO0, acc[0][i]);
      acc[1][i] = MFMA16(wO[i], hO1, acc[1][i]);
      if (pf && (i & 1) == 0) piece(st + 3, i >> 1);
    }
    __builtin_amdgcn_sched_group_barrier(0x100, 4, 0);
#pragma unroll
    for (int i = 0; i < 8; ++i) { __builtin_amdgcn_sched_group_barrier(0x8, 2, 0); __builtin_amdgcn_sched_group_barrier(0x100, 2, 0); }
    __builtin_amdgcn_sched_group_barrier(0x8, 2, 0);
    __builtin_amdgcn_sched_barrier(0);
  };
#pragma unroll 1
  for (int st = 0; st < 14; st += 2) {
    WAIT_VM(10); body(st, true, wA, hA0, hA1, wB, hB0, hB1);
    WAIT_VM(10); body(st + 1, true, wB, hB0, hB1, wA, hA0, hA1);
  }
  WAIT_VM(10); body(14, true, wA, hA0, hA1, wB, hB0, hB1);
  WAIT_VM(10); body(15, false, wB, hB0, hB1, wA, hA0, hA1);
  WAIT_VM(5); body(16, false, wA, hA0, hA1, wB, hB0, hB1);
  WAIT_VM(0); body(17, false, wB, hB0, hB1, wA, hA0, hA1);
#pragma unroll
  for (int i = 0; i < 9; ++i) { acc[0][i] = MFMA16(wB[i], hB0, acc[0][i]); acc[1][i] = MFMA16(wB[i], hB1, acc[1][i]); }
  BARRIER();
  if (kh == 1) {
#pragma unroll
    for (int a = 0; a < 2; ++a)
#pragma unroll
      for (int i = 0; i < 9; ++i) *(f32x4*)(smem + ((tg * 18 + a * 9 + i) * 64 + lane) * 16) = acc[a][i];
  }
  BARRIER();
  if (kh == 0) {
    float* P = (float*)(ws + OFF_P);
#pragma unroll
    for (int a = 0; a < 2; ++a)
#pragma unroll
      for (int i = 0; i < 9; ++i) {
        f32x4 o = *(const f32x4*)(smem + ((tg * 18 + a * 9 + i) * 64 + lane) * 16);
        *(f32x4*)(P + ((((size_t)sp * 128 + tt0 + a) * 36 + nb * 9 + i) * 64 + lane) * 4) = acc[a][i] + o;
      }
  }
}

__global__ __launch_bounds__(256) void k5_out(const char* __restrict__ ws, u16* __restrict__ out) {
  const int id = blockIdx.x * 256 + threadIdx.x;
  const int lane = id & 63, fr = id >> 6, nt = fr % 36, tt = fr / 36;
  const float* P = (const float*)(ws + OFF_P);
  f32x4 s = *(const f32x4*)(P + (size_t)id * 4);
#pragma unroll
  for (int sp = 1; sp < 4; ++sp) s += *(const f32x4*)(P + ((size_t)sp * 128 * 36 * 64 + id) * 4);
  const u32x2 xv = *(const u32x2*)((const u16*)(ws + OFF_X1) + (size_t)id * 4);
  const int n0 = nt * 16 + (lane >> 4) * 4;
  const f32x4 gb = *(const f32x4*)((const float*)(ws + OFF_MODF) + 5 * 576 + n0);
  unsigned d01 = pk2(s[0], s[1]), d23 = pk2(s[2], s[3]);
  unsigned y01 = pk2(gb[0] * lo2f(d01), gb[1] * hi2f(d01)), y23 = pk2(gb[2] * lo2f(d23), gb[3] * hi2f(d23));
  unsigned z01 = pk2(lo2f(xv[0]) + lo2f(y01), hi2f(xv[0]) + hi2f(y01));
  unsigned z23 = pk2(lo2f(xv[1]) + lo2f(y23), hi2f(xv[1]) + hi2f(y23));
  *(u32x2*)(out + (size_t)(tt * 16 + (lane & 15)) * D_ + n0) = (u32x2){z01, z23};
}

extern "C" void kernel_launch(void* const* d_in, const int* in_sizes, int n_in, void* d_out, int out_size, void* d_ws, size_t ws_size,
                              hipStream_t stream) {
  const u16* x = (const u16*)d_in[0];
  const u16* wqkv = (const u16*)d_in[1];
  const u16* wphi = (const u16*)d_in[2];
  const u16* wo = (const u16*)d_in[3];
  const u16* wg = (const u16*)d_in[5];
  const u16* wu = (const u16*)d_in[6];
  const u16* wd = (const u16*)d_in[7];
  const u16* c = (const u16*)d_in[8];
  const u16* wmod = (const u16*)d_in[9];
  char* ws = (char*)d_ws;
  (void)hipFuncSetAttribute((const void*)k1_qkv, hipFuncAttributeMaxDynamicSharedMemorySize, K1_LDS);
  (void)hipFuncSetAttribute((const void*)k2_attn, hipFuncAttributeMaxDynamicSharedMemorySize, K2_LDS);
  (void)hipFuncSetAttribute((const void*)k3_ffn1, hipFuncAttributeMaxDynamicSharedMemorySize, 4 * K3_SLOT);
  (void)hipFuncSetAttribute((const void*)k4_ffn2, hipFuncAttributeMaxDynamicSharedMemorySize, 4 * K4_SLOT);
  k0_prep<<<K0_BLOCKS, 256, 0, stream>>>(x, wqkv, wphi, wo, c, wmod, ws);
  k1_qkv<<<256, 512, K1_LDS, stream>>>(ws, wg, wu, wd);
  k2_attn<<<256, 576, K2_LDS, stream>>>(x, ws);
  k3_ffn1<<<256, 512, 4 * K3_SLOT, stream>>>(wg, wu, ws);
  k4_ffn2<<<256, 512, 4 * K4_SLOT, stream>>>(wd, ws);
  k5_out<<<1152, 256, 0, stream>>>(ws, (u16*)d_out);
}
```

```cpp
#include <hip/hip_runtime.h>
#include <hip/hip_bf16.h>
#include <stdint.h>

typedef unsigned short u16;
typedef __attribute__((ext_vector_type(8))) short bf16x8;
typedef __attribute__((ext_vector_type(4))) short s16x4;
typedef __attribute__((ext_vector_type(4))) float f32x4;
typedef __attribute__((ext_vector_type(2))) float f32x2;
typedef __attribute__((ext_vector_type(4))) unsigned u32x4;
typedef __attribute__((ext_vector_type(2))) unsigned u32x2;
typedef __attribute__((ext_vector_type(2))) __bf16 bf16x2_t;
typedef __attribute__((address_space(3))) s16x4* lds_s16x4_p;
typedef __attribute__((address_space(1))) unsigned long long gu64;
typedef __attribute__((address_space(1))) unsigned gu32;

#define DI __device__ __forceinline__
#define MFMA16(a, b, c) __builtin_amdgcn_mfma_f32_16x16x32_bf16((a), (b), (c), 0, 0, 0)
#define MFMA32(a, b, c) __builtin_amdgcn_mfma_f32_32x32x16_bf16((a), (b), (c), 0, 0, 0)
typedef __attribute__((ext_vector_type(16))) float f32x16;

constexpr int S_ = 2048, D_ = 576, DF_ = 4608;

constexpr size_t OFF_MODF = 0;
constexpr size_t OFF_KV   = 16384;
constexpr size_t OFF_CNT  = OFF_KV + 51200;
constexpr size_t OFF_CNT3 = OFF_CNT + 2048;
constexpr size_t OFF_CNT4 = OFF_CNT3 + 32 * 256;
constexpr size_t SZ_KV    = 51200 + 2048 + 96 * 256;
constexpr size_t OFF_STAT = OFF_KV + SZ_KV;
constexpr size_t SZ_STAT  = 64 * 4 * 64 * 8;
constexpr size_t OFF_XN1  = OFF_STAT + SZ_STAT;
constexpr size_t SZ_ACT   = (size_t)128 * 18 * 1024;
constexpr size_t OFF_WQ   = OFF_XN1 + SZ_ACT;
constexpr size_t SZ_WQ    = (size_t)24 * 18 * 5 * 1024;
constexpr size_t OFF_WPHI = OFF_WQ + SZ_WQ;
constexpr size_t SZ_WPHI  = (size_t)8 * 15 * 1024;
constexpr size_t OFF_WO   = OFF_WPHI + SZ_WPHI;
constexpr size_t SZ_WO    = (size_t)36 * 18 * 1024;
constexpr size_t OFF_PHIQ = OFF_WO + SZ_WO;
constexpr size_t SZ_PHIQ  = (size_t)128 * 40 * 1024;
constexpr size_t OFF_H2   = OFF_PHIQ + SZ_PHIQ;
constexpr size_t OFF_X1   = OFF_H2 + SZ_ACT;
constexpr size_t OFF_HH   = OFF_X1 + SZ_ACT;
constexpr size_t SZ_HH    = (size_t)128 * 144 * 1024;
constexpr size_t OFF_P    = OFF_HH + SZ_HH;
constexpr size_t OFF_PK   = OFF_P + SZ_HH;

#define DBG_INIT(k)
#define STAMP(i)
#define RSTAMP(i)
DI unsigned pk2(float a, float b) {
  f32x2 v = {a, b};
  bf16x2_t r = __builtin_convertvector(v, bf16x2_t);
  return __builtin_bit_cast(unsigned, r);
}
DI float lo2f(unsigned u) { return __uint_as_float(u << 16); }
DI float hi2f(unsigned u) { return __uint_as_float(u & 0xffff0000u); }
DI float bf2f(u16 v) { return __uint_as_float(((unsigned)v) << 16); }
DI float rbf(float f) { return lo2f(pk2(f, f)); }
DI float fdiv(float a, float b, float rb) {
  float q = a * rb;
  float r = __builtin_fmaf(-q, b, a);
  return __builtin_fmaf(r, rb, q);
}
DI void glds16(unsigned voff, const void* sbase, unsigned lds_dst) {
  asm volatile("s_mov_b32 m0, %2\n\ts_nop 0\n\tglobal_load_lds_dwordx4 %0, %1"
               :: "v"(voff), "s"(sbase), "s"(lds_dst));
}
DI void glds16c(unsigned voff, const void* sbase, unsigned lds_dst) {
  asm volatile("s_mov_b32 m0, %2\n\ts_nop 0\n\tglobal_load_lds_dwordx4 %0, %1 sc1"
               :: "v"(voff), "s"(sbase), "s"(lds_dst));
}
DI void glds16q(unsigned voff, const void* sbase, unsigned lds_dst) {
  unsigned long long sv;
  asm volatile("s_mov_b64 %0, exec\n\ts_mov_b64 exec, 0xffff\n\ts_mov_b32 m0, %3\n\ts_nop 0\n\tglobal_load_lds_dwordx4 %1, %2\n\ts_mov_b64 exec, %0"
               : "=&s"(sv) : "v"(voff), "s"(sbase), "s"(lds_dst));
}
#define WAIT_VM(n) asm volatile("s_waitcnt vmcnt(" #n ")" ::: "memory")
#define WAIT_LGKM0() asm volatile("s_waitcnt lgkmcnt(0)" ::: "memory")
#define BARRIER() do { asm volatile("s_waitcnt lgkmcnt(0)" ::: "memory"); __builtin_amdgcn_s_barrier(); asm volatile("" ::: "memory"); } while (0)

DI bf16x8 tr_frag(unsigned a0, unsigned a1) {
  s16x4 lo = __builtin_amdgcn_ds_read_tr16_b64_v4i16((lds_s16x4_p)(size_t)a0);
  s16x4 hi = __builtin_amdgcn_ds_read_tr16_b64_v4i16((lds_s16x4_p)(size_t)a1);
  return __builtin_shufflevector(lo, hi, 0, 1, 2, 3, 4, 5, 6, 7);
}
#define WS_RSRC __builtin_amdgcn_make_buffer_rsrc((void*)ws, 0, 0x10000000, 0x00020000)
#define st16_wt(p, v) __builtin_amdgcn_raw_buffer_store_b128((v), WS_RSRC, (unsigned)((const char*)(p) - (const char*)ws), 0, 16)
#define st8_wt(p, ...) __builtin_amdgcn_raw_buffer_store_b64((__VA_ARGS__), WS_RSRC, (unsigned)((const char*)(p) - (const char*)ws), 0, 16)
DI float sum_g4(float v) {
  unsigned u = __float_as_uint(v);
  auto r = __builtin_amdgcn_permlane16_swap(u, u, false, false);
  float a = __uint_as_float(r[0]) + __uint_as_float(r[1]);
  unsigned w = __float_as_uint(a);
  auto r2 = __builtin_amdgcn_permlane32_swap(w, w, false, false);
  return __uint_as_float(r2[0]) + __uint_as_float(r2[1]);
}
DI const void* uniform_ptr(const void* p) {
  unsigned long long v = (unsigned long long)p;
  unsigned lo = __builtin_amdgcn_readfirstlane((unsigned)v), hi = __builtin_amdgcn_readfirstlane((unsigned)(v >> 32));
  return (const void*)(((unsigned long long)hi << 32) | lo);
}

constexpr int K0_GEMV = 108, K0_XN = 64, K0_WQ = 135, K0_WO = 41, K0_WPHI = 8, K0_ZERO = 13;
constexpr int K0_BLOCKS = K0_GEMV + K0_XN + K0_WQ + K0_WO + K0_WPHI + K0_ZERO;

__global__ __launch_bounds__(256) void k0_prep(const u16* __restrict__ x, const u16* __restrict__ wqkv, const u16* __restrict__ wphi,
                                               const u16* __restrict__ wo, const u16* __restrict__ cvec, const u16* __restrict__ wmod,
                                               char* __restrict__ ws) {
  __shared__ float red[4][32];
  const int tid = threadIdx.x, lane = tid & 63, wave = tid >> 6;
  int b = blockIdx.x;
  DBG_INIT(0);
  if (b < K0_GEMV) {
    int cgp = b;
    if (b < 96) { int grp = b >> 4, r = b & 15; cgp = 2 * (grp * 8 + (r & 7)) + (r >> 3); }
    const int gq = tid & 3, rg = tid >> 2;
    u32x4 w[9];
    float sc[9];
#pragma unroll
    for (int i = 0; i < 9; ++i) w[i] = *(const u32x4*)(wmod + (size_t)(rg + 64 * i) * 3456 + cgp * 32 + gq * 8);
#pragma unroll
    for (int i = 0; i < 9; ++i) { float cv = bf2f(cvec[rg + 64 * i]); sc[i] = rbf(cv / (1.f + __expf(-cv))); }
    float acc[8];
#pragma unroll
    for (int j = 0; j < 8; ++j) acc[j] = 0.f;
#pragma unroll
    for (int i = 0; i < 9; ++i)
#pragma unroll
      for (int j = 0; j < 4; ++j) { acc[2 * j] += sc[i] * lo2f(w[i][j]); acc[2 * j + 1] += sc[i] * hi2f(w[i][j]); }
#pragma unroll
    for (int j = 0; j < 8; ++j) {
      float v = acc[j];
      v += __shfl_xor(v, 4); v += __shfl_xor(v, 8); v += __shfl_xor(v, 16); v += __shfl_xor(v, 32);
      acc[j] = v;
    }
    if (lane < 4) {
#pragma unroll
      for (int j = 0; j < 8; ++j) red[wave][lane * 8 + j] = acc[j];
    }
    __syncthreads();
    if (tid < 32) {
      float v = rbf(red[0][tid] + red[1][tid] + red[2][tid] + red[3][tid]);
      int n = cgp * 32 + tid, part = n / 576;
      if (part == 1 || part == 4) v = rbf(1.f + v);
      ((float*)(ws + OFF_MODF))[n] = v;
    }
    STAMP(1); RSTAMP(61);
    return;
  }
  b -= K0_GEMV;
  if (b < K0_XN) {
    const int l8 = lane & 7, rr = lane >> 3, row = (b * 4 + wave) * 8 + rr;
    u32x4 v[9];
#pragma unroll
    for (int i = 0; i < 9; ++i) v[i] = *(const u32x4*)(x + (size_t)row * D_ + (8 * i + l8) * 8);
    float s = 0.f;
#pragma unroll
    for (int i = 0; i < 9; ++i)
#pragma unroll
      for (int j = 0; j < 4; ++j) s += lo2f(v[i][j]) + hi2f(v[i][j]);
    s += __shfl_xor(s, 1); s += __shfl_xor(s, 2); s += __shfl_xor(s, 4);
    const float mean = s * (1.f / 576.f);
    float q = 0.f;
#pragma unroll
    for (int i = 0; i < 9; ++i)
#pragma unroll
      for (int j = 0; j < 4; ++j) { float a = lo2f(v[i][j]) - mean, c = hi2f(v[i][j]) - mean; q += a * a + c * c; }
    q += __shfl_xor(q, 1); q += __shfl_xor(q, 2); q += __shfl_xor(q, 4);
    const float mb = rbf(mean), sd = rbf(sqrtf(rbf(rbf(q * (1.f / 576.f)) + 1e-5f))), rsd = 1.f / sd;
    u16* xn = (u16*)(ws + OFF_XN1);
    const int tt = row >> 4, t = row & 15;
#pragma unroll
    for (int i = 0; i < 9; ++i) {
      u32x4 o;
#pragma unroll
      for (int j = 0; j < 4; ++j) {
        unsigned d = pk2(lo2f(v[i][j]) - mb, hi2f(v[i][j]) - mb);
        o[j] = pk2(fdiv(lo2f(d), sd, rsd), fdiv(hi2f(d), sd, rsd));
      }
      int kg = 8 * i + l8, ks = kg >> 2, g = kg & 3;
      st16_wt(xn + ((size_t)(tt * 18 + ks) * 64 + g * 16 + t) * 8, o);
    }
    STAMP(1); RSTAMP(61);
    return;
  }
  b -= K0_XN;
  if (b < K0_WQ) {
    u16* dst = (u16*)(ws + OFF_WQ);
    u32x4 ov[4];
#pragma unroll
    for (int it = 0; it < 4; ++it) {
      int id = (b * 4 + it) * 256 + tid;
      int ln = id & 63, frag = id >> 6, tile = frag % 5, ks = (frag / 5) % 18, unit = frag / 90;
      int g = ln >> 4, m = ln & 15, slot = 4 * tile + (m & 3), hm = m >> 2;
      int col = (unit >> 3) * 576 + (4 * (unit & 7) + hm) * 18 + slot;
      const unsigned msk = slot < 18 ? 0xffffffffu : 0u;
      const u16* p = wqkv + (size_t)(32 * ks + 8 * g) * 1728 + (slot < 18 ? col : 0);
      u32x4 o;
#pragma unroll
      for (int j = 0; j < 4; ++j) o[j] = ((unsigned)p[(2 * j) * 1728] | ((unsigned)p[(2 * j + 1) * 1728] << 16)) & msk;
      ov[it] = o;
    }
#pragma unroll
    for (int it = 0; it < 4; ++it) {
      int id = (b * 4 + it) * 256 + tid;
      u32x4 o = ov[it];
      st16_wt(dst + (size_t)id * 8, o);
    }
    STAMP(1); RSTAMP(61);
    return;
  }
  b -= K0_WQ;
  if (b < K0_WO) {
    u16* dst = (u16*)(ws + OFF_WO);
    u32x4 ov[4];
#pragma unroll
    for (int it = 0; it < 4; ++it) {
      int id = min((b * 4 + it) * 256 + tid, 648 * 64 - 1);
      int ln = id & 63, frag = id >> 6, ks = frag % 18, nt = frag / 18;
      int g = ln >> 4, m = ln & 15;
#pragma unroll
      for (int j = 0; j < 4; ++j) {
        int p0 = 8 * ks + 2 * j, u0 = p0 / 18, d0 = p0 % 18;
        const u16* p = wo + (size_t)((4 * u0 + g) * 18 + d0) * 576 + nt * 16 + m;
        ov[it][j] = (unsigned)p[0] | ((unsigned)p[576] << 16);
      }
    }
#pragma unroll
    for (int it = 0; it < 4; ++it) {
      int id = (b * 4 + it) * 256 + tid;
      if (id < 648 * 64) st16_wt(dst + (size_t)id * 8, ov[it]);
    }
    STAMP(1); RSTAMP(61);
    return;
  }
  b -= K0_WO;
  if (b < K0_WPHI) {
    u16* dst = (u16*)(ws + OFF_WPHI);
    u32x4 ov[4];
#pragma unroll
    for (int it = 0; it < 4; ++it) {
      int id = min((b * 4 + it) * 256 + tid, 120 * 64 - 1);
      int ln = id & 63, frag = id >> 6, ks = frag % 3, tile = (frag / 3) % 5, u = frag / 15;
      int g = ln >> 4, m = ln & 15, f = 4 * tile + (m & 3), hm = m >> 2;
#pragma unroll
      for (int j = 0; j < 4; ++j) {
        int d = 8 * ks + 2 * j;
        const bool ok = hm == g && f < 18 && d < 18;
        const u16* p = wphi + (size_t)((4 * u + g) * 18 + (ok ? d : 0)) * 18 + (ok ? f : 0);
        ov[it][j] = ((unsigned)p[0] | ((unsigned)p[18] << 16)) & (ok ? 0xffffffffu : 0u);
      }
    }
#pragma unroll
    for (int it = 0; it < 4; ++it) {
      int id = (b * 4 + it) * 256 + tid;
      if (id < 120 * 64) st16_wt(dst + (size_t)id * 8, ov[it]);
    }
    STAMP(1); RSTAMP(61);
    return;
  }
  b -= K0_WPHI;
  {
    u32x4 z = {0u, 0u, 0u, 0u};
#pragma unroll
    for (int it = 0; it < 4; ++it) {
      int id = (b * 4 + it) * 256 + tid;
      if (id < (int)((SZ_KV + SZ_STAT) / 16)) st16_wt(ws + OFF_KV + (size_t)id * 16, z);
    }
    STAMP(1); RSTAMP(61);
  }
}

constexpr int K1_CH = 45 * 1024;
constexpr int K1_MOD = 3 * K1_CH;
constexpr int K1_PHI = K1_MOD + 4608;
constexpr int K1_DUM = K1_PHI + 15360;
constexpr int K1_LDS = K1_DUM + 1024;
constexpr int PHI_STR = 352, V_STR = 224, V_OFF = 64 * PHI_STR;

DI void feature_map(const f32x4 (&acc)[5], const char* wphi_lds, u32x4 (&phi)[5]) {
  bf16x8 qf[3];
  {
    u32x4 a0 = {pk2(acc[0][0], acc[0][1]), pk2(acc[0][2], acc[0][3]), pk2(acc[1][0], acc[1][1]), pk2(acc[1][2], acc[1][3])};
    u32x4 a1 = {pk2(acc[2][0], acc[2][1]), pk2(acc[2][2], acc[2][3]), pk2(acc[3][0], acc[3][1]), pk2(acc[3][2], acc[3][3])};
    u32x4 a2 = {pk2(acc[4][0], acc[4][1]), 0u, 0u, 0u};
    qf[0] = __builtin_bit_cast(bf16x8, a0); qf[1] = __builtin_bit_cast(bf16x8, a1); qf[2] = __builtin_bit_cast(bf16x8, a2);
  }
  f32x4 c[5];
#pragma unroll
  for (int i = 0; i < 5; ++i) c[i] = (f32x4){0.f, 0.f, 0.f, 0.f};
#pragma unroll
  for (int ks = 0; ks < 3; ++ks)
#pragma unroll
    for (int i = 0; i < 5; ++i) {
      bf16x8 w = *(const bf16x8*)(wphi_lds + (i * 3 + ks) * 1024);
      c[i] = MFMA16(w, qf[ks], c[i]);
    }
  float p[20];
#pragma unroll
  for (int i = 0; i < 5; ++i)
#pragma unroll
    for (int r = 0; r < 4; r += 2) { unsigned u = pk2(c[i][r], c[i][r + 1]); p[4 * i + r] = lo2f(u); p[4 * i + r + 1] = hi2f(u); }
  float mx = fmaxf(p[0], p[1]), mn = fminf(p[0], p[1]);
#pragma unroll
  for (int f = 2; f < 18; f += 2) { mx = __builtin_fmaxf(__builtin_fmaxf(mx, p[f]), p[f + 1]); mn = __builtin_fminf(__builtin_fminf(mn, p[f]), p[f + 1]); }
  const float L2E = 1.4426950408889634f;
  const float mxs = -mx * L2E, mns = mn * L2E;
  float e1[18], e2[18], s1 = 0.f, s2 = 0.f;
#pragma unroll
  for (int f = 0; f < 18; ++f) {
    e1[f] = __builtin_amdgcn_exp2f(__builtin_fmaf(p[f], L2E, mxs)); s1 += e1[f];
    e2[f] = __builtin_amdgcn_exp2f(__builtin_fmaf(p[f], -L2E, mns)); s2 += e2[f];
  }
  const float r1 = __builtin_amdgcn_rcpf(s1), r2 = __builtin_amdgcn_rcpf(s2);
  unsigned o[20];
#pragma unroll
  for (int f = 0; f < 18; f += 2) {
    o[f >> 1] = pk2(e1[f] * r1, e1[f + 1] * r1);
    o[9 + (f >> 1)] = pk2(e2[f] * r2, e2[f + 1] * r2);
  }
  o[18] = 0u; o[19] = 0u;
#pragma unroll
  for (int i = 0; i < 5; ++i) { phi[i][0] = o[4 * i]; phi[i][1] = o[4 * i + 1]; phi[i][2] = o[4 * i + 2]; phi[i][3] = o[4 * i + 3]; }
}

__global__ __launch_bounds__(320) void k1_qkv(char* __restrict__ ws, const u16* __restrict__ wg, const u16* __restrict__ wu, const u16* __restrict__ wd) {
  extern __shared__ __attribute__((aligned(16))) char smem[];
  const int tid = threadIdx.x, lane = tid & 63;
  const int wave = __builtin_amdgcn_readfirstlane(tid >> 6);
  const int b = blockIdx.x, xcd = b & 7, idx = b >> 3;
  if (wave == 4) {
#pragma unroll
    for (int i = 0; i < 5; ++i) __builtin_amdgcn_s_barrier();
    const unsigned sink = (unsigned)(size_t)smem + K1_DUM;
#pragma unroll
    for (int m = 0; m < 3; ++m) {
      const char* p = (const char*)(m == 0 ? wd : (m == 1 ? wg : wu)) + (size_t)b * 20736;
#pragma unroll
      for (int i = 0; i < 20; ++i) glds16((unsigned)lane * 16u, p + i * 1024, sink);
      glds16q((unsigned)lane * 16u, p + 20480, sink);
    }
#pragma unroll
    for (int i = 0; i < 6; ++i) __builtin_amdgcn_s_barrier();
    WAIT_VM(0);
    return;
  }
  const int mt = xcd * 4 + (idx >> 3), j = idx & 7;
  const int tt = mt * 4 + wave, g = lane >> 4, t = lane & 15;
  const unsigned lds0 = (unsigned)(size_t)smem;
  const unsigned vo = (unsigned)lane * 16u;
  DBG_INIT(1);
  const char* wsrc = ws + OFF_WQ + (size_t)j * 18 * 5120 + wave * 4096;
  const unsigned wdst = lds0 + wave * 4096;
  const bool w3 = wave == 3;
  auto issue_unit = [&](int c, int u) __attribute__((always_inline)) {
    const char* sp = wsrc + (size_t)u * 8 * 18 * 5120 + c * 15360;
    const unsigned dp = wdst + (c % 3) * K1_CH + u * 15360;
#pragma unroll
    for (int i = 0; i < 3; ++i) glds16(vo, sp + i * 1024, dp + i * 1024);
    if (!w3) glds16(vo, sp + 3072, dp + 3072);
  };
  issue_unit(0, 0); issue_unit(0, 1); issue_unit(0, 2);
  u32x4 xn[18];
  const u16* xp = (const u16*)(ws + OFF_XN1) + ((size_t)tt * 18 * 64 + lane) * 8;
  {
    const float* mf = (const float*)(ws + OFF_MODF);
    const f32x4 mv0 = *(const f32x4*)(mf + (tid < 144 ? 576 + tid * 4 : (tid - 144) * 4));
    const f32x4 mv1 = *(const f32x4*)(mf + (112 + (tid & 31)) * 4);
#pragma unroll
    for (int ks = 0; ks < 12; ++ks) xn[ks] = *(const u32x4*)(xp + (size_t)ks * 512);
    *(f32x4*)(smem + K1_MOD + tid * 16) = mv0;
    if (tid < 32) *(f32x4*)(smem + K1_MOD + (256 + tid) * 16) = mv1;
  }
  STAMP(1);
  WAIT_VM(0);
#pragma unroll
  for (int ks = 0; ks < 12; ++ks) asm volatile("" : "+v"(xn[ks]));
  STAMP(2);
  BARRIER();
  STAMP(3);
  const float* modl = (const float*)(smem + K1_MOD) + g * 8;
  auto modulate = [&](int ks) __attribute__((always_inline)) {
    const f32x4 c1a = *(const f32x4*)(modl + ks * 32), c1b = *(const f32x4*)(modl + ks * 32 + 4);
    const f32x4 saa = *(const f32x4*)(modl + 576 + ks * 32), sab = *(const f32x4*)(modl + 576 + ks * 32 + 4);
    unsigned q0 = pk2(lo2f(xn[ks][0]) * c1a[0], hi2f(xn[ks][0]) * c1a[1]);
    unsigned q1 = pk2(lo2f(xn[ks][1]) * c1a[2], hi2f(xn[ks][1]) * c1a[3]);
    unsigned q2 = pk2(lo2f(xn[ks][2]) * c1b[0], hi2f(xn[ks][2]) * c1b[1]);
    unsigned q3 = pk2(lo2f(xn[ks][3]) * c1b[2], hi2f(xn[ks][3]) * c1b[3]);
    asm volatile("" : "+v"(q0), "+v"(q1), "+v"(q2), "+v"(q3));
    xn[ks][0] = pk2(lo2f(q0) + saa[0], hi2f(q0) + saa[1]);
    xn[ks][1] = pk2(lo2f(q1) + saa[2], hi2f(q1) + saa[3]);
    xn[ks][2] = pk2(lo2f(q2) + sab[0], hi2f(q2) + sab[1]);
    xn[ks][3] = pk2(lo2f(q3) + sab[2], hi2f(q3) + sab[3]);
    asm volatile("" : "+v"(xn[ks]));
  };
  issue_unit(1, 0); issue_unit(1, 1); issue_unit(1, 2);
  modulate(0);
  bf16x8 w[3][5];
  auto loadw = [&](int ks, int u) __attribute__((always_inline)) {
    const char* wb = smem + ((ks / 3) % 3) * K1_CH + u * 15360 + (ks % 3) * 5120 + lane * 16;
#pragma unroll
    for (int i = 0; i < 5; ++i) w[u][i] = *(const bf16x8*)(wb + i * 1024);
  };
  loadw(0, 0); loadw(0, 1); loadw(0, 2);
  __builtin_amdgcn_sched_barrier(0);
  STAMP(4);
  f32x4 acc[3][5];
#pragma unroll
  for (int u = 0; u < 3; ++u)
#pragma unroll
    for (int i = 0; i < 5; ++i) acc[u][i] = (f32x4){0.f, 0.f, 0.f, 0.f};
#pragma unroll
  for (int ks = 0; ks < 18; ++ks) {
    if (ks % 3 == 2 && ks < 17) {
      if (ks == 2) WAIT_VM(9); else if (ks == 5) WAIT_VM(12); else if (ks == 14) WAIT_VM(0); else WAIT_VM(9);
      BARRIER();
    }
    const bf16x8 hf = __builtin_bit_cast(bf16x8, xn[ks]);
    f32x4 c1a, c1b, saa, sab;
    unsigned q0, q1, q2, q3;
    if (ks < 17) {
      c1a = *(const f32x4*)(modl + (ks + 1) * 32); c1b = *(const f32x4*)(modl + (ks + 1) * 32 + 4);
      saa = *(const f32x4*)(modl + 576 + (ks + 1) * 32); sab = *(const f32x4*)(modl + 576 + (ks + 1) * 32 + 4);
    }
#pragma unroll
    for (int u = 0; u < 3; ++u) {
#pragma unroll
      for (int i = 0; i < 5; ++i) acc[u][i] = MFMA16(w[u][i], hf, acc[u][i]);
      if (ks < 17) loadw(ks + 1, u);
      if (ks < 17 && u == 1) {
        const u32x4 xx = xn[ks + 1];
        q0 = pk2(lo2f(xx[0]) * c1a[0], hi2f(xx[0]) * c1a[1]);
        q1 = pk2(lo2f(xx[1]) * c1a[2], hi2f(xx[1]) * c1a[3]);
        q2 = pk2(lo2f(xx[2]) * c1b[0], hi2f(xx[2]) * c1b[1]);
        q3 = pk2(lo2f(xx[3]) * c1b[2], hi2f(xx[3]) * c1b[3]);
        asm volatile("" : "+v"(q0), "+v"(q1), "+v"(q2), "+v"(q3));
      }
      if (ks < 17 && u == 2) {
        xn[ks + 1][0] = pk2(lo2f(q0) + saa[0], hi2f(q0) + saa[1]);
        xn[ks + 1][1] = pk2(lo2f(q1) + saa[2], hi2f(q1) + saa[3]);
        xn[ks + 1][2] = pk2(lo2f(q2) + sab[0], hi2f(q2) + sab[1]);
        xn[ks + 1][3] = pk2(lo2f(q3) + sab[2], hi2f(q3) + sab[3]);
        asm volatile("" : "+v"(xn[ks + 1]));
      }
      if (ks == 0) issue_unit(2, u);
      if (ks == 6) { xn[12 + 2 * u] = *(const u32x4*)(xp + (size_t)(12 + 2 * u) * 512); xn[13 + 2 * u] = *(const u32x4*)(xp + (size_t)(13 + 2 * u) * 512); }
      if (ks == 3 && u == 0) {
        const char* ps = ws + OFF_WPHI + (size_t)j * 15360 + wave * 4096;
#pragma unroll
        for (int i = 0; i < 3; ++i) glds16(vo, ps + i * 1024, lds0 + K1_PHI + wave * 4096 + i * 1024);
        if (!w3) glds16(vo, ps + 3072, lds0 + K1_PHI + wave * 4096 + 3072);
      }
      if (ks % 3 == 2 && ks < 9) issue_unit(ks / 3 + 3, u);
      if (ks < 17 && u > 0) {
#pragma unroll
        for (int i = 0; i < 5; ++i) {
          __builtin_amdgcn_sched_group_barrier(0x8, 1, 0);
          __builtin_amdgcn_sched_group_barrier(0x2, 3, 0);
          __builtin_amdgcn_sched_group_barrier(0x100, 1, 0);
        }
      }
      __builtin_amdgcn_sched_barrier(0);
    }
  }
  STAMP(5);
  {
    u32x4 phi[5];
    feature_map(acc[1], smem + K1_PHI + lane * 16, phi);
#pragma unroll
    for (int i = 0; i < 5; ++i) asm volatile("" : "+v"(phi[i]));
    STAMP(6);
    BARRIER();
    const int tk = wave * 16 + t;
    char* pr = smem + tk * PHI_STR + g * 80;
#pragma unroll
    for (int i = 0; i < 5; ++i) *(u32x4*)(pr + i * 16) = phi[i];
    char* vr = smem + V_OFF + tk * V_STR + g * 40;
#pragma unroll
    for (int i = 0; i < 4; ++i) *(u32x2*)(vr + i * 8) = (u32x2){pk2(acc[2][i][0], acc[2][i][1]), pk2(acc[2][i][2], acc[2][i][3])};
    *(u32x2*)(vr + 32) = (u32x2){pk2(acc[2][4][0], acc[2][4][1]), pk2(1.f, 0.f)};
  }
  BARRIER();
  STAMP(7);
  {
    const int hd = wave, q4 = (lane & 15) >> 2, p = lane & 3;
    f32x4 kacc[2][3];
#pragma unroll
    for (int m = 0; m < 2; ++m)
#pragma unroll
      for (int n = 0; n < 3; ++n) kacc[m][n] = (f32x4){0.f, 0.f, 0.f, 0.f};
    const unsigned va = lds0 + V_OFF + (4 * g + q4) * V_STR + hd * 40 + p * 8;
    const unsigned pa = lds0 + (4 * g + q4) * PHI_STR + hd * 80 + p * 8;
#pragma unroll
    for (int ks = 0; ks < 2; ++ks) {
      bf16x8 a0 = tr_frag(va + (32 * ks) * V_STR, va + (32 * ks + 16) * V_STR);
      bf16x8 a1 = tr_frag(va + (32 * ks) * V_STR + 32, va + (32 * ks + 16) * V_STR + 32);
#pragma unroll
      for (int n = 0; n < 3; ++n) {
        bf16x8 bb = tr_frag(pa + (32 * ks) * PHI_STR + n * 32, pa + (32 * ks + 16) * PHI_STR + n * 32);
        kacc[0][n] = MFMA16(a0, bb, kacc[0][n]);
        kacc[1][n] = MFMA16(a1, bb, kacc[1][n]);
      }
    }
    STAMP(8);
    float* pk = (float*)(ws + OFF_PK) + ((size_t)((j * 32 + mt) * 4 + hd) * 6 * 64 + lane) * 4;
#pragma unroll
    for (int m = 0; m < 2; ++m)
#pragma unroll
      for (int n = 0; n < 3; ++n) st16_wt(pk + (m * 3 + n) * 256, __builtin_bit_cast(u32x4, kacc[m][n]));
    WAIT_VM(0);
  }
  BARRIER();
  gu32* cnt = (gu32*)(ws + OFF_CNT + j * 256);
  if (tid == 0) __hip_atomic_fetch_add(cnt, 1u, __ATOMIC_RELAXED, __HIP_MEMORY_SCOPE_AGENT);
  STAMP(9);
  {
    u16* pq = (u16*)(ws + OFF_PHIQ);
    u32x4 phi[5];
    feature_map(acc[0], smem + K1_PHI + lane * 16, phi);
#pragma unroll
    for (int i = 0; i < 5; ++i) st16_wt(pq + (((size_t)(tt * 8 + j) * 5 + i) * 64 + lane) * 8, phi[i]);
  }
  STAMP(10);
  if (wave == 0) {
    for (unsigned spins = 0; spins < (1u << 22); ++spins) {
      if (__builtin_amdgcn_readfirstlane(__hip_atomic_load(cnt, __ATOMIC_RELAXED, __HIP_MEMORY_SCOPE_AGENT)) >= 32u) break;
      __builtin_amdgcn_s_sleep(1);
    }
  }
  BARRIER();
  STAMP(11);
  {
    const int pos = tid % 48, pg = tid / 48;
    f32x4 sum = {0.f, 0.f, 0.f, 0.f};
    if (tid < 192) {
      const float* src = (const float*)(ws + OFF_PK) + ((size_t)(j * 32 + pg * 8) * 1536 + mt * 48 + pos) * 4;
      f32x4 v[8];
#pragma unroll
      for (int i = 0; i < 8; ++i) asm volatile("global_load_dwordx4 %0, %1, off sc1" : "=v"(v[i]) : "v"(src + (size_t)i * 6144));
      asm volatile("s_waitcnt vmcnt(0)" : "+v"(v[0]), "+v"(v[1]), "+v"(v[2]), "+v"(v[3]), "+v"(v[4]), "+v"(v[5]), "+v"(v[6]), "+v"(v[7]) :: "memory");
      sum = ((v[0] + v[1]) + (v[2] + v[3])) + ((v[4] + v[5]) + (v[6] + v[7]));
      *(f32x4*)(smem + tid * 16) = sum;
    }
    BARRIER();
    if (tid < 48) {
      const f32x4* r = (const f32x4*)smem + tid;
      sum = (r[0] + r[48]) + (r[96] + r[144]);
      const int q = mt * 48 + tid;
      const int hd = q / 384, mn = (q % 384) >> 6, ln = q & 63, m = mn / 3, n = mn % 3;
      const int f = n * 16 + (ln & 15), d0 = m * 16 + (ln >> 4) * 4;
      u16* kvb = (u16*)(ws + OFF_KV) + (size_t)(4 * j + hd) * 800 + d0 * 40 + f;
      if (f < 36) {
#pragma unroll
        for (int r2 = 0; r2 < 4; ++r2) if (d0 + r2 < 19) kvb[r2 * 40] = (u16)(pk2(sum[r2], 0.f) & 0xffffu);
      }
    }
  }
  STAMP(12); RSTAMP(61);
}

constexpr int K2_KV = 36864, K2_ZERO = K2_KV + 51200, K2_ST = K2_ZERO + 1600, K2_LDS = K2_ST + 9 * 32 * 8 + 32 * 8;

__global__ __launch_bounds__(576) void k2_attn(const u16* __restrict__ x, char* __restrict__ ws) {
  extern __shared__ __attribute__((aligned(16))) char smem[];
  const int tid = threadIdx.x, lane = tid & 63;
  const int wave = __builtin_amdgcn_readfirstlane(tid >> 6);
  const int b = blockIdx.x, xcd = b & 7, idx = b >> 3;
  const int rt = xcd * 8 + (idx >> 2), cb = idx & 3;
  const int g = lane >> 4, t = lane & 15;
  const int nt = cb * 9 + wave;
  DBG_INIT(2);
  {
    const unsigned lds0 = (unsigned)(size_t)smem;
    const char* ks = ws + OFF_KV + wave * 6144;
#pragma unroll
    for (int i = 0; i < 2; ++i) glds16((unsigned)lane * 16u, ks + i * 1024, lds0 + K2_KV + wave * 6144 + i * 1024);
    if (wave < 8) {
#pragma unroll
      for (int i = 2; i < 6; ++i) glds16((unsigned)lane * 16u, ks + i * 1024, lds0 + K2_KV + wave * 6144 + i * 1024);
    }
    if (tid < 100) *(u32x4*)(smem + K2_ZERO + tid * 16) = (u32x4){0u, 0u, 0u, 0u};
  }
  bf16x8 ph[2][5];
  {
    const int u = wave & 7;
    const u16* pq = (const u16*)(ws + OFF_PHIQ);
#pragma unroll
    for (int a = 0; a < 2; ++a)
#pragma unroll
      for (int s = 0; s < 5; ++s) ph[a][s] = *(const bf16x8*)(pq + (((size_t)((rt * 2 + a) * 8 + u) * 5 + s) * 64 + lane) * 8);
  }
  bf16x8 wf[18];
  const u16* wp = (const u16*)(ws + OFF_WO) + ((size_t)nt * 18 * 64 + lane) * 8;
  STAMP(1);
  WAIT_VM(10);
  BARRIER();
  STAMP(2);
  if (wave < 8) {
    const int u = wave;
    f32x4 oacc[2][5];
#pragma unroll
    for (int a = 0; a < 2; ++a)
#pragma unroll
      for (int i = 0; i < 5; ++i) oacc[a][i] = (f32x4){0.f, 0.f, 0.f, 0.f};
    const char* kb = smem + (((t >> 2) == g) ? K2_KV + (4 * u + g) * 1600 + (t & 3) * 80 : K2_ZERO);
#pragma unroll
    for (int s = 0; s < 5; ++s) {
#pragma unroll
      for (int i = 0; i < 5; ++i) {
        const bf16x8 a8 = *(const bf16x8*)(kb + i * 320 + s * 16);
        oacc[0][i] = MFMA16(a8, ph[0][s], oacc[0][i]);
        oacc[1][i] = MFMA16(a8, ph[1][s], oacc[1][i]);
      }
      if (s >= 1 && s <= 3) {
#pragma unroll
        for (int q = 0; q < 6; ++q) wf[(s - 1) * 6 + q] = *(const bf16x8*)(wp + (size_t)((s - 1) * 6 + q) * 512);
      }
      __builtin_amdgcn_sched_barrier(0);
    }
#pragma unroll
    for (int a = 0; a < 2; ++a) {
      const float den = rbf(rbf(oacc[a][4][2]) + 1e-6f), rden = __builtin_amdgcn_rcpf(den);
#pragma unroll
      for (int d = 0; d < 18; d += 2) {
        unsigned nq = pk2(oacc[a][d >> 2][d & 3], oacc[a][d >> 2][(d & 3) + 1]);
        unsigned o = pk2(fdiv(lo2f(nq), den, rden), fdiv(hi2f(nq), den, rden));
        const int pos = 18 * u + d;
        *(unsigned*)(smem + ((a * 18 + (pos >> 3)) * 64 + lane) * 16 + (pos & 7) * 2) = o;
      }
    }
  }
  else {
#pragma unroll
    for (int ks = 0; ks < 18; ++ks) wf[ks] = *(const bf16x8*)(wp + (size_t)ks * 512);
  }
  const float* mf = (const float*)(ws + OFF_MODF);
  const int n0 = nt * 16 + 4 * g;
  const f32x4 ga = *(const f32x4*)(mf + 2 * 576 + n0), sb = *(const f32x4*)(mf + 3 * 576 + n0), c1b = *(const f32x4*)(mf + 4 * 576 + n0);
  u32x2 xv[2];
#pragma unroll
  for (int a = 0; a < 2; ++a) xv[a] = *(const u32x2*)(x + (size_t)((rt * 2 + a) * 16 + t) * D_ + n0);
  STAMP(3);
  __syncthreads();
  STAMP(4);
  f32x4 acc[2] = {{0.f, 0.f, 0.f, 0.f}, {0.f, 0.f, 0.f, 0.f}}, acc2[2] = {{0.f, 0.f, 0.f, 0.f}, {0.f, 0.f, 0.f, 0.f}};
#pragma unroll
  for (int ks = 0; ks < 18; ks += 2)
#pragma unroll
    for (int a = 0; a < 2; ++a) {
      bf16x8 of = *(const bf16x8*)(smem + ((a * 18 + ks) * 64 + lane) * 16);
      bf16x8 of2 = *(const bf16x8*)(smem + ((a * 18 + ks + 1) * 64 + lane) * 16);
      acc[a] = MFMA16(wf[ks], of, acc[a]);
      acc2[a] = MFMA16(wf[ks + 1], of2, acc2[a]);
    }
  acc[0] += acc2[0]; acc[1] += acc2[1];
  float x1[2][4];
  float* st = (float*)(smem + K2_ST);
  float* fin = st + 9 * 32 * 2;
#pragma unroll
  for (int a = 0; a < 2; ++a) {
    unsigned a01 = pk2(acc[a][0], acc[a][1]), a23 = pk2(acc[a][2], acc[a][3]);
    unsigned y01 = pk2(ga[0] * lo2f(a01), ga[1] * hi2f(a01)), y23 = pk2(ga[2] * lo2f(a23), ga[3] * hi2f(a23));
    unsigned z01 = pk2(lo2f(xv[a][0]) + lo2f(y01), hi2f(xv[a][0]) + hi2f(y01));
    unsigned z23 = pk2(lo2f(xv[a][1]) + lo2f(y23), hi2f(xv[a][1]) + hi2f(y23));
    x1[a][0] = lo2f(z01); x1[a][1] = hi2f(z01); x1[a][2] = lo2f(z23); x1[a][3] = hi2f(z23);
    st8_wt((u16*)(ws + OFF_X1) + (((size_t)(rt * 2 + a) * 36 + nt) * 64 + lane) * 4, (u32x2){z01, z23});
    float s = (x1[a][0] + x1[a][1]) + (x1[a][2] + x1[a][3]);
    s = sum_g4(s);
    const float mw = s * (1.f / 16.f);
    float q = 0.f;
#pragma unroll
    for (int r = 0; r < 4; ++r) { float d = x1[a][r] - mw; q += d * d; }
    q = sum_g4(q);
    if (g == 0) *(f32x2*)(st + (wave * 32 + a * 16 + t) * 2) = (f32x2){mw, q};
  }
  STAMP(5);
  __syncthreads();
  STAMP(6);
  if (wave == 0) {
    const int tok = lane & 31;
    float mw[9], s = 0.f, q = 0.f;
#pragma unroll
    for (int w = 0; w < 9; ++w) { f32x2 v = *(const f32x2*)(st + (w * 32 + tok) * 2); mw[w] = v[0]; s += v[0]; q += v[1]; }
    const float mb = s * (1.f / 9.f);
    float dq = 0.f;
#pragma unroll
    for (int w = 0; w < 9; ++w) { float d = mw[w] - mb; dq += d * d; }
    q += 16.f * dq;
    const float mine = lane < 32 ? mb : q;
    gu64* G = (gu64*)(ws + OFF_STAT) + (size_t)rt * 4 * 64;
    __hip_atomic_store(G + cb * 64 + lane, (1ull << 32) | (unsigned long long)__float_as_uint(mine), __ATOMIC_RELAXED, __HIP_MEMORY_SCOPE_AGENT);
    float vals[4];
    for (unsigned spins = 0; spins < (1u << 22); ++spins) {
      bool ok = true;
#pragma unroll
      for (int c = 0; c < 4; ++c) {
        unsigned long long v = __hip_atomic_load(G + c * 64 + lane, __ATOMIC_RELAXED, __HIP_MEMORY_SCOPE_AGENT);
        vals[c] = __uint_as_float((unsigned)v);
        ok &= (v >> 32) == 1ull;
      }
      if (__all(ok)) break;
      __builtin_amdgcn_s_sleep(1);
    }
    float m2 = (vals[0] + vals[1]) + (vals[2] + vals[3]);
    const float mean = __shfl(m2, tok) * 0.25f;
    float dd = 0.f;
#pragma unroll
    for (int c = 0; c < 4; ++c) { float d = vals[c] - mean; dd += d * d; }
    const float M2 = __shfl(m2, 32 + tok) + 144.f * dd;
    if (lane < 32) {
      const float mbf = rbf(mean), sd = rbf(sqrtf(rbf(rbf(M2 * (1.f / 576.f)) + 1e-5f)));
      *(f32x2*)(fin + lane * 2) = (f32x2){mbf, sd};
    }
  }
  STAMP(7);
  __syncthreads();
  STAMP(8);
#pragma unroll
  for (int a = 0; a < 2; ++a) {
    const f32x2 ms = *(const f32x2*)(fin + (a * 16 + t) * 2);
    const float rsd = 1.f / ms[1];
    unsigned d01 = pk2(x1[a][0] - ms[0], x1[a][1] - ms[0]), d23 = pk2(x1[a][2] - ms[0], x1[a][3] - ms[0]);
    unsigned n01 = pk2(fdiv(lo2f(d01), ms[1], rsd), fdiv(hi2f(d01), ms[1], rsd));
    unsigned n23 = pk2(fdiv(lo2f(d23), ms[1], rsd), fdiv(hi2f(d23), ms[1], rsd));
    unsigned m01 = pk2(lo2f(n01) * c1b[0], hi2f(n01) * c1b[1]), m23 = pk2(lo2f(n23) * c1b[2], hi2f(n23) * c1b[3]);
    unsigned h01 = pk2(lo2f(m01) + sb[0], hi2f(m01) + sb[1]), h23 = pk2(lo2f(m23) + sb[2], hi2f(m23) + sb[3]);
    st8_wt((u16*)(ws + OFF_H2) + (((size_t)rt * 36 + nt) * 64 + (g & 1) * 32 + a * 16 + t) * 8 + (g >> 1) * 4, (u32x2){h01, h23});
  }
  STAMP(9); RSTAMP(61);
}

constexpr int K3_UOFF = 9216 + 128, K3_AOFF = 18688, K3_SLOT = K3_AOFF + 16384;

DI float geglu(float gv, float uv) {
  const float k0 = -2.302208198f;
  const float k1 = -0.1029432397f;
  float tq = gv * gv;
  float pz = __builtin_fmaf(tq, k1, k0);
  float e = __builtin_amdgcn_exp2f(gv * pz);
  float r = __builtin_amdgcn_rcpf(1.f + e);
  return gv * r * uv;
}

constexpr int K4_SLOT = 18432 + 16384;

__global__ __launch_bounds__(512, 2) void k3_ffn(const u16* __restrict__ wg, const u16* __restrict__ wu, const u16* __restrict__ wd, char* __restrict__ ws, u16* __restrict__ out) {
  extern __shared__ __attribute__((aligned(16))) char smem[];
  const int tid = threadIdx.x, lane = tid & 63;
  const int wave = __builtin_amdgcn_readfirstlane(tid >> 6);
  const int b = blockIdx.x, xcd = b & 7, idx = b >> 3;
  {
  const int ntile = xcd * 4 + (idx & 3), mtile = idx >> 2;
  const unsigned lds0 = (unsigned)(size_t)smem;
  const int T = mtile * 8 + wave;
  DBG_INIT(3);
  const int gl0 = wave * 64 + lane, gl1 = (wave == 0 ? 512 : (wave - 1) * 64) + lane, gl2 = 448 + wave * 16 + (lane & 15);
  const unsigned vo0 = (unsigned)((gl0 / 18) * (DF_ * 2) + (gl0 % 18) * 16);
  const unsigned vo1 = (unsigned)((gl1 / 18) * (DF_ * 2) + (gl1 % 18) * 16);
  const unsigned vo2 = (unsigned)((gl2 / 18) * (DF_ * 2) + (gl2 % 18) * 16);
  const unsigned voa = (unsigned)lane * 16u;
  const char* sg = (const char*)wg + ntile * 288;
  const char* su = (const char*)wu + ntile * 288;
  const char* s1 = wave == 0 ? sg : su;
  const char* sa = ws + OFF_H2 + (size_t)T * 36 * 1024;
  const unsigned d0 = lds0 + wave * 1024, d1 = lds0 + (wave == 0 ? 8192 : K3_UOFF + (wave - 1) * 1024);
  const unsigned d2 = lds0 + K3_UOFF + 7168 + wave * 256, d3 = lds0 + K3_AOFF + wave * 2048;
  auto piece = [&](int st, int i) {
    const size_t dl = (size_t)st * 32 * (DF_ * 2);
    const unsigned sl = (st & 3) * K3_SLOT;
    if (i == 0) glds16(vo0, sg + dl, d0 + sl);
    else if (i == 1) glds16(vo1, s1 + dl, d1 + sl);
    else if (i == 2) glds16q(vo2, su + dl, d2 + sl);
    else if (i == 3) glds16(voa, sa + (size_t)st * 2048, d3 + sl);
    else glds16(voa, sa + (size_t)st * 2048 + 1024, d3 + sl + 1024);
  };
#pragma unroll
  for (int st = 0; st < 3; ++st)
#pragma unroll
    for (int i = 0; i < 5; ++i) piece(st, i);
  f32x16 acc[9];
#pragma unroll
  for (int i = 0; i < 9; ++i)
#pragma unroll
    for (int r = 0; r < 16; ++r) acc[i][r] = 0.f;
  const int h = lane >> 5, grp = (lane >> 4) & 1, q4 = (lane & 15) >> 2, p = lane & 3;
  const unsigned rbase = lds0 + (grp ? K3_UOFF : 0) + (4 * h + q4) * 288 + p * 8;
  const unsigned abase = K3_AOFF + wave * 2048 + lane * 16;
  bf16x8 wA[9], wB[9], aA, aB;
#pragma unroll
  for (int i = 0; i < 9; ++i) wB[i] = (bf16x8){0, 0, 0, 0, 0, 0, 0, 0};
  aB = (bf16x8){0, 0, 0, 0, 0, 0, 0, 0};
  auto body = [&](int st, bool pf) __attribute__((always_inline)) {
    BARRIER();
    const unsigned sl = (st & 3) * K3_SLOT;
    aA = *(const bf16x8*)(smem + sl + abase);
#pragma unroll
    for (int tn = 0; tn < 9; ++tn) {
      const unsigned ad = rbase + sl + tn * 32;
      wA[tn] = tr_frag(ad, ad + 8 * 288);
      acc[tn] = MFMA32(wB[tn], aB, acc[tn]);
      if (pf && (tn == 1 || tn == 4 || tn == 7)) piece(st + 3, tn / 3);
    }
    __builtin_amdgcn_sched_group_barrier(0x100, 3, 0);
#pragma unroll
    for (int tn = 0; tn < 8; ++tn) { __builtin_amdgcn_sched_group_barrier(0x8, 1, 0); __builtin_amdgcn_sched_group_barrier(0x100, 2, 0); }
    __builtin_amdgcn_sched_group_barrier(0x8, 1, 0);
    __builtin_amdgcn_sched_barrier(0);
    aB = *(const bf16x8*)(smem + sl + abase + 1024);
#pragma unroll
    for (int tn = 0; tn < 9; ++tn) {
      const unsigned ad = rbase + sl + 16 * 288 + tn * 32;
      wB[tn] = tr_frag(ad, ad + 8 * 288);
      acc[tn] = MFMA32(wA[tn], aA, acc[tn]);
      if (pf && (tn == 1 || tn == 4)) piece(st + 3, 3 + tn / 3);
    }
    __builtin_amdgcn_sched_group_barrier(0x100, 3, 1);
#pragma unroll
    for (int tn = 0; tn < 8; ++tn) { __builtin_amdgcn_sched_group_barrier(0x8, 1, 1); __builtin_amdgcn_sched_group_barrier(0x100, 2, 1); }
    __builtin_amdgcn_sched_group_barrier(0x8, 1, 1);
    __builtin_amdgcn_sched_barrier(0);
  };
  STAMP(1);
  WAIT_VM(10); body(0, true);
  STAMP(2);
#define PFV true
#pragma unroll 1
  for (int st = 1; st < 15; ++st) { WAIT_VM(10); body(st, PFV); }
  STAMP(3);
  WAIT_VM(10); body(15, false);
  WAIT_VM(5); body(16, false);
  WAIT_VM(0); body(17, false);
#pragma unroll
  for (int tn = 0; tn < 9; ++tn) acc[tn] = MFMA32(wB[tn], aB, acc[tn]);
  STAMP(4);
  u16* hh = (u16*)(ws + OFF_HH);
  const int t32 = lane & 31;
#pragma unroll
  for (int tn = 0; tn < 9; ++tn) {
    u32x4 o;
#pragma unroll
    for (int j = 0; j < 4; ++j) o[j] = pk2(geglu(acc[tn][2 * j], acc[tn][8 + 2 * j]), geglu(acc[tn][2 * j + 1], acc[tn][9 + 2 * j]));
    const int ft = ntile * 9 + tn;
    st16_wt(hh + (((size_t)(2 * T + (t32 >> 4)) * 144 + (ft >> 1)) * 64 + (2 * (ft & 1) + h) * 16 + (t32 & 15)) * 8, o);
  }
  STAMP(5);
  WAIT_VM(0);
  BARRIER();
  if (tid == 0) __hip_atomic_fetch_add((gu32*)(ws + OFF_CNT3 + (mtile * 4 + (ntile >> 3)) * 256), 1u, __ATOMIC_RELAXED, __HIP_MEMORY_SCOPE_AGENT);
  STAMP(6);
  }
  const int sp = xcd >> 1, mtile = (xcd & 1) * 8 + (idx >> 2), nb = idx & 3;
  const int tg = wave & 3, kh = wave >> 2;
  const int g = lane >> 4, q4 = (lane & 15) >> 2, p = lane & 3;
  const unsigned lds0 = (unsigned)(size_t)smem;
  const int tt0 = mtile * 8 + tg * 2;
  const int gl0 = tid, gl1 = 512 + tid, gl2 = 1024 + wave * 16 + (lane & 15);
  const unsigned vo0 = (unsigned)((gl0 / 18) * (D_ * 2) + (gl0 % 18) * 16);
  const unsigned vo1 = (unsigned)((gl1 / 18) * (D_ * 2) + (gl1 % 18) * 16);
  const unsigned vo2 = (unsigned)((gl2 / 18) * (D_ * 2) + (gl2 % 18) * 16);
  const unsigned voa = (unsigned)lane * 16u;
  const char* sw = (const char*)wd + (size_t)(1152 * sp) * (D_ * 2) + nb * 288;
  const char* sh = ws + OFF_HH + ((size_t)tt0 * 144 + 36 * sp + kh) * 1024;
  const unsigned d0 = lds0 + wave * 1024, d2 = lds0 + 16384 + wave * 256, d3 = lds0 + 18432 + wave * 2048;
  auto piece = [&](int st, int i) {
    const size_t dl = (size_t)st * 64 * (D_ * 2);
    const unsigned sl = (st & 3) * K4_SLOT;
    if (i == 0) glds16(vo0, sw + dl, d0 + sl);
    else if (i == 1) glds16(vo1, sw + dl, d0 + sl + 8192);
    else if (i == 2) glds16q(vo2, sw + dl, d2 + sl);
    else if (i == 3) glds16c(voa, sh + (size_t)st * 2048, d3 + sl);
    else glds16c(voa, sh + (size_t)st * 2048 + 144 * 1024, d3 + sl + 1024);
  };
#pragma unroll
  for (int st = 0; st < 3; ++st)
#pragma unroll
    for (int i = 0; i < 3; ++i) piece(st, i);
  if (wave == 0) {
    gu32* c3 = (gu32*)(ws + OFF_CNT3 + ((mtile >> 1) * 4 + sp) * 256);
    for (unsigned spins = 0; spins < (1u << 22); ++spins) {
      if (__builtin_amdgcn_readfirstlane(__hip_atomic_load(c3, __ATOMIC_RELAXED, __HIP_MEMORY_SCOPE_AGENT)) >= 8u) break;
      __builtin_amdgcn_s_sleep(1);
    }
    STAMP(13);
  }
  BARRIER();
  STAMP(7);
#pragma unroll
  for (int st = 0; st < 3; ++st)
#pragma unroll
    for (int i = 3; i < 5; ++i) piece(st, i);
  f32x4 acc[2][9];
#pragma unroll
  for (int a = 0; a < 2; ++a)
#pragma unroll
    for (int i = 0; i < 9; ++i) acc[a][i] = (f32x4){0.f, 0.f, 0.f, 0.f};
  const unsigned rbase = lds0 + (32 * kh + 16 * (g >> 1) + 4 * (g & 1) + q4) * 288 + p * 8;
  const unsigned abase = 18432 + wave * 2048 + lane * 16;
  bf16x8 wA[9], wB[9], hA0, hA1, hB0, hB1;
#pragma unroll
  for (int i = 0; i < 9; ++i) wB[i] = (bf16x8){0, 0, 0, 0, 0, 0, 0, 0};
  hB0 = (bf16x8){0, 0, 0, 0, 0, 0, 0, 0}; hB1 = hB0;
  auto body = [&](int st, bool pf, bf16x8 (&wN)[9], bf16x8& hN0, bf16x8& hN1, bf16x8 (&wO)[9], bf16x8& hO0, bf16x8& hO1) __attribute__((always_inline)) {
    BARRIER();
    const unsigned sl = (st & 3) * K4_SLOT;
    hN0 = *(const bf16x8*)(smem + sl + abase);
    hN1 = *(const bf16x8*)(smem + sl + abase + 1024);
#pragma unroll
    for (int i = 0; i < 9; ++i) {
      const unsigned ad = rbase + sl + i * 32;
      wN[i] = tr_frag(ad, ad + 8 * 288);
      acc[0][i] = MFMA16(wO[i], hO0, acc[0][i]);
      acc[1][i] = MFMA16(wO[i], hO1, acc[1][i]);
      if (pf && (i & 1) == 0) piece(st + 3, i >> 1);
    }
    __builtin_amdgcn_sched_group_barrier(0x100, 4, 0);
#pragma unroll
    for (int i = 0; i < 8; ++i) { __builtin_amdgcn_sched_group_barrier(0x8, 2, 0); __builtin_amdgcn_sched_group_barrier(0x100, 2, 0); }
    __builtin_amdgcn_sched_group_barrier(0x8, 2, 0);
    __builtin_amdgcn_sched_barrier(0);
  };
  WAIT_VM(4); body(0, true, wA, hA0, hA1, wB, hB0, hB1);
  WAIT_VM(7); body(1, true, wB, hB0, hB1, wA, hA0, hA1);
  STAMP(8);
#pragma unroll 1
  for (int st = 2; st < 14; st += 2) {
    WAIT_VM(10); body(st, true, wA, hA0, hA1, wB, hB0, hB1);
    WAIT_VM(10); body(st + 1, true, wB, hB0, hB1, wA, hA0, hA1);
  }
  WAIT_VM(10); body(14, true, wA, hA0, hA1, wB, hB0, hB1);
  WAIT_VM(10); body(15, false, wB, hB0, hB1, wA, hA0, hA1);
  WAIT_VM(5); body(16, false, wA, hA0, hA1, wB, hB0, hB1);
  WAIT_VM(0); body(17, false, wB, hB0, hB1, wA, hA0, hA1);
#pragma unroll
  for (int i = 0; i < 9; ++i) { acc[0][i] = MFMA16(wB[i], hB0, acc[0][i]); acc[1][i] = MFMA16(wB[i], hB1, acc[1][i]); }
  BARRIER();
  STAMP(9);
  {
    char* xb = smem + (((tg * 2 + (1 - kh)) * 9) * 64 + lane) * 16;
#pragma unroll
    for (int i = 0; i < 9; ++i) *(f32x4*)(xb + i * 1024) = kh ? acc[0][i] : acc[1][i];
  }
  BARRIER();
  float* P = (float*)(ws + OFF_P);
  {
    const char* xb = smem + (((tg * 2 + kh) * 9) * 64 + lane) * 16;
    f32x4 r[9];
#pragma unroll
    for (int i = 0; i < 9; ++i) r[i] = (kh ? acc[1][i] : acc[0][i]) + *(const f32x4*)(xb + i * 1024);
    if (tg != sp) {
      const unsigned po = (unsigned)(OFF_P + (((size_t)(sp * 128 + tt0 + kh) * 4 + nb) * 5 * 64 + lane) * 16);
#pragma unroll
      for (int i = 0; i < 5; ++i) {
        u32x4 o = {pk2(r[2 * i][0], r[2 * i][1]), pk2(r[2 * i][2], r[2 * i][3]), 0u, 0u};
        if (i < 4) { o[2] = pk2(r[2 * i + 1][0], r[2 * i + 1][1]); o[3] = pk2(r[2 * i + 1][2], r[2 * i + 1][3]); }
        __builtin_amdgcn_raw_buffer_store_b128(o, WS_RSRC, po + i * 1024, 0, 16);
      }
      WAIT_VM(0);
    } else {
#pragma unroll
      for (int i = 0; i < 9; ++i) *(f32x4*)(smem + 73728 + ((kh * 9 + i) * 64 + lane) * 16) = r[i];
    }
  }
  BARRIER();
  gu32* c4 = (gu32*)(ws + OFF_CNT4 + (mtile * 4 + nb) * 256);
  if (tid == 0) __hip_atomic_fetch_add(c4, 1u, __ATOMIC_RELAXED, __HIP_MEMORY_SCOPE_AGENT);
  STAMP(10);
  {
    const int ttb = mtile * 8 + sp * 2;
    const float* mf = (const float*)(ws + OFF_MODF) + 5 * 576 + nb * 144 + g * 4;
    u32x2 xv[2][2];
    f32x4 gb[2][2];
#pragma unroll
    for (int q = 0; q < 2; ++q) {
      const int e = min(wave + 8 * q, 9), a = e / 5, pi = e % 5;
#pragma unroll
      for (int hh = 0; hh < 2; ++hh) {
        const int i = min(2 * pi + hh, 8);
        xv[q][hh] = *(const u32x2*)((const u16*)(ws + OFF_X1) + (((size_t)(ttb + a) * 36 + nb * 9 + i) * 64 + lane) * 4);
        gb[q][hh] = *(const f32x4*)(mf + i * 16);
      }
    }
    if (wave == 0) {
      for (unsigned spins = 0; spins < (1u << 22); ++spins) {
        if (__builtin_amdgcn_readfirstlane(__hip_atomic_load(c4, __ATOMIC_RELAXED, __HIP_MEMORY_SCOPE_AGENT)) >= 4u) break;
        __builtin_amdgcn_s_sleep(1);
      }
    }
    BARRIER();
    STAMP(11);
    u32x4 pv[2][3];
#pragma unroll
    for (int q = 0; q < 2; ++q) {
      const int e = min(wave + 8 * q, 9), a = e / 5, pi = e % 5;
#pragma unroll
      for (int o = 0; o < 3; ++o) {
        const int so = o + (o >= sp ? 1 : 0);
        pv[q][o] = __builtin_amdgcn_raw_buffer_load_b128(WS_RSRC,
                     (unsigned)(OFF_P + ((((size_t)(so * 128 + ttb + a) * 4 + nb) * 5 + pi) * 64 + lane) * 16), 0, 16);
      }
    }
#pragma unroll
    for (int q = 0; q < 2; ++q) {
      const int e = wave + 8 * q;
      if (e < 10) {
        const int a = e / 5, pi = e % 5;
#pragma unroll
        for (int hh = 0; hh < 2; ++hh) {
          const int i = 2 * pi + hh;
          if (i < 9) {
            const f32x4 own = *(const f32x4*)(smem + 73728 + ((a * 9 + i) * 64 + lane) * 16);
            f32x4 pf[3];
#pragma unroll
            for (int o = 0; o < 3; ++o) pf[o] = (f32x4){lo2f(pv[q][o][2 * hh]), hi2f(pv[q][o][2 * hh]), lo2f(pv[q][o][2 * hh + 1]), hi2f(pv[q][o][2 * hh + 1])};
            const f32x4 p0 = sp == 0 ? own : pf[0];
            const f32x4 p1 = sp == 1 ? own : (sp < 1 ? pf[0] : pf[1]);
            const f32x4 p2 = sp == 2 ? own : (sp < 2 ? pf[1] : pf[2]);
            const f32x4 p3 = sp == 3 ? own : pf[2];
            const f32x4 sm = ((p0 + p1) + p2) + p3;
            unsigned d01 = pk2(sm[0], sm[1]), d23 = pk2(sm[2], sm[3]);
            unsigned y01 = pk2(gb[q][hh][0] * lo2f(d01), gb[q][hh][1] * hi2f(d01)), y23 = pk2(gb[q][hh][2] * lo2f(d23), gb[q][hh][3] * hi2f(d23));
            unsigned z01 = pk2(lo2f(xv[q][hh][0]) + lo2f(y01), hi2f(xv[q][hh][0]) + hi2f(y01));
            unsigned z23 = pk2(lo2f(xv[q][hh][1]) + lo2f(y23), hi2f(xv[q][hh][1]) + hi2f(y23));
            *(u32x2*)(out + (size_t)((ttb + a) * 16 + (lane & 15)) * D_ + nb * 144 + i * 16 + g * 4) = (u32x2){z01, z23};
          }
        }
      }
    }
  }
  STAMP(12); RSTAMP(61);
}

extern "C" void kernel_launch(void* const* d_in, const int* in_sizes, int n_in, void* d_out, int out_size, void* d_ws, size_t ws_size,
                              hipStream_t stream) {
  const u16* x = (const u16*)d_in[0];
  const u16* wqkv = (const u16*)d_in[1];
  const u16* wphi = (const u16*)d_in[2];
  const u16* wo = (const u16*)d_in[3];
  const u16* wg = (const u16*)d_in[5];
  const u16* wu = (const u16*)d_in[6];
  const u16* wd = (const u16*)d_in[7];
  const u16* c = (const u16*)d_in[8];
  const u16* wmod = (const u16*)d_in[9];
  char* ws = (char*)d_ws;
  (void)hipFuncSetAttribute((const void*)k1_qkv, hipFuncAttributeMaxDynamicSharedMemorySize, K1_LDS);
  (void)hipFuncSetAttribute((const void*)k2_attn, hipFuncAttributeMaxDynamicSharedMemorySize, K2_LDS);
  (void)hipFuncSetAttribute((const void*)k3_ffn, hipFuncAttributeMaxDynamicSharedMemorySize, 4 * K3_SLOT);
  k0_prep<<<K0_BLOCKS, 256, 0, stream>>>(x, wqkv, wphi, wo, c, wmod, ws);
  k1_qkv<<<256, 320, K1_LDS, stream>>>(ws, wg, wu, wd);
  k2_attn<<<256, 576, K2_LDS, stream>>>(x, ws);
  k3_ffn<<<256, 512, 4 * K3_SLOT, stream>>>(wg, wu, wd, ws, (u16*)d_out);
}
```

```cpp
#include <hip/hip_runtime.h>
#include <hip/hip_bf16.h>
#include <stdint.h>

typedef unsigned short u16;
typedef __attribute__((ext_vector_type(8))) short bf16x8;
typedef __attribute__((ext_vector_type(4))) short s16x4;
typedef __attribute__((ext_vector_type(4))) float f32x4;
typedef __attribute__((ext_vector_type(2))) float f32x2;
typedef __attribute__((ext_vector_type(4))) unsigned u32x4;
typedef __attribute__((ext_vector_type(2))) unsigned u32x2;
typedef __attribute__((ext_vector_type(2))) __bf16 bf16x2_t;
typedef __attribute__((address_space(3))) s16x4* lds_s16x4_p;
typedef __attribute__((address_space(1))) unsigned long long gu64;
typedef __attribute__((address_space(1))) unsigned gu32;

#define DI __device__ __forceinline__
#define MFMA16(a, b, c) __builtin_amdgcn_mfma_f32_16x16x32_bf16((a), (b), (c), 0, 0, 0)
#define MFMA32(a, b, c) __builtin_amdgcn_mfma_f32_32x32x16_bf16((a), (b), (c), 0, 0, 0)
typedef __attribute__((ext_vector_type(16))) float f32x16;

constexpr int S_ = 2048, D_ = 576, DF_ = 4608;

constexpr size_t OFF_MODF = 0;
constexpr size_t OFF_KV   = 16384;
constexpr size_t OFF_CNT  = OFF_KV + 51200;
constexpr size_t OFF_CNT3 = OFF_CNT + 2048;
constexpr size_t OFF_CNT4 = OFF_CNT3 + 32 * 256;
constexpr size_t SZ_KV    = 51200 + 2048 + 96 * 256;
constexpr size_t OFF_STAT = OFF_KV + SZ_KV;
constexpr size_t SZ_STAT  = 64 * 4 * 64 * 8;
constexpr size_t OFF_XN1  = OFF_STAT + SZ_STAT;
constexpr size_t SZ_ACT   = (size_t)128 * 18 * 1024;
constexpr size_t OFF_WQ   = OFF_XN1 + SZ_ACT;
constexpr size_t SZ_WQ    = (size_t)24 * 18 * 5 * 1024;
constexpr size_t OFF_WPHI = OFF_WQ + SZ_WQ;
constexpr size_t SZ_WPHI  = (size_t)8 * 15 * 1024;
constexpr size_t OFF_WO   = OFF_WPHI + SZ_WPHI;
constexpr size_t SZ_WO    = (size_t)36 * 18 * 1024;
constexpr size_t OFF_PHIQ = OFF_WO + SZ_WO;
constexpr size_t SZ_PHIQ  = (size_t)128 * 40 * 1024;
constexpr size_t OFF_H2   = OFF_PHIQ + SZ_PHIQ;
constexpr size_t OFF_X1   = OFF_H2 + SZ_ACT;
constexpr size_t OFF_HH   = OFF_X1 + SZ_ACT;
constexpr size_t SZ_HH    = (size_t)128 * 144 * 1024;
constexpr size_t OFF_P    = OFF_HH + SZ_HH;
constexpr size_t OFF_PK   = OFF_P + SZ_HH;

#define DBG_INIT(k)
#define STAMP(i)
#define RSTAMP(i)
DI unsigned pk2(float a, float b) {
  f32x2 v = {a, b};
  bf16x2_t r = __builtin_convertvector(v, bf16x2_t);
  return __builtin_bit_cast(unsigned, r);
}
DI float lo2f(unsigned u) { return __uint_as_float(u << 16); }
DI float hi2f(unsigned u) { return __uint_as_float(u & 0xffff0000u); }
DI float bf2f(u16 v) { return __uint_as_float(((unsigned)v) << 16); }
DI float rbf(float f) { return lo2f(pk2(f, f)); }
DI float fdiv(float a, float b, float rb) {
  float q = a * rb;
  float r = __builtin_fmaf(-q, b, a);
  return __builtin_fmaf(r, rb, q);
}
DI void glds16(unsigned voff, const void* sbase, unsigned lds_dst) {
  asm volatile("s_mov_b32 m0, %2\n\ts_nop 0\n\tglobal_load_lds_dwordx4 %0, %1"
               :: "v"(voff), "s"(sbase), "s"(lds_dst));
}
DI void glds16c(unsigned voff, const void* sbase, unsigned lds_dst) {
  asm volatile("s_mov_b32 m0, %2\n\ts_nop 0\n\tglobal_load_lds_dwordx4 %0, %1 sc1"
               :: "v"(voff), "s"(sbase), "s"(lds_dst));
}
DI void glds16q(unsigned voff, const void* sbase, unsigned lds_dst) {
  unsigned long long sv;
  asm volatile("s_mov_b64 %0, exec\n\ts_mov_b64 exec, 0xffff\n\ts_mov_b32 m0, %3\n\ts_nop 0\n\tglobal_load_lds_dwordx4 %1, %2\n\ts_mov_b64 exec, %0"
               : "=&s"(sv) : "v"(voff), "s"(sbase), "s"(lds_dst));
}
#define WAIT_VM(n) asm volatile("s_waitcnt vmcnt(" #n ")" ::: "memory")
#define WAIT_LGKM0() asm volatile("s_waitcnt lgkmcnt(0)" ::: "memory")
#define BARRIER() do { asm volatile("s_waitcnt lgkmcnt(0)" ::: "memory"); __builtin_amdgcn_s_barrier(); asm volatile("" ::: "memory"); } while (0)

DI bf16x8 tr_frag(unsigned a0, unsigned a1) {
  s16x4 lo = __builtin_amdgcn_ds_read_tr16_b64_v4i16((lds_s16x4_p)(size_t)a0);
  s16x4 hi = __builtin_amdgcn_ds_read_tr16_b64_v4i16((lds_s16x4_p)(size_t)a1);
  return __builtin_shufflevector(lo, hi, 0, 1, 2, 3, 4, 5, 6, 7);
}
#define WS_RSRC __builtin_amdgcn_make_buffer_rsrc((void*)ws, 0, 0x10000000, 0x00020000)
#define st16_wt(p, v) __builtin_amdgcn_raw_buffer_store_b128((v), WS_RSRC, (unsigned)((const char*)(p) - (const char*)ws), 0, 16)
#define st8_wt(p, ...) __builtin_amdgcn_raw_buffer_store_b64((__VA_ARGS__), WS_RSRC, (unsigned)((const char*)(p) - (const char*)ws), 0, 16)
DI float sum_g4(float v) {
  unsigned u = __float_as_uint(v);
  auto r = __builtin_amdgcn_permlane16_swap(u, u, false, false);
  float a = __uint_as_float(r[0]) + __uint_as_float(r[1]);
  unsigned w = __float_as_uint(a);
  auto r2 = __builtin_amdgcn_permlane32_swap(w, w, false, false);
  return __uint_as_float(r2[0]) + __uint_as_float(r2[1]);
}
DI const void* uniform_ptr(const void* p) {
  unsigned long long v = (unsigned long long)p;
  unsigned lo = __builtin_amdgcn_readfirstlane((unsigned)v), hi = __builtin_amdgcn_readfirstlane((unsigned)(v >> 32));
  return (const void*)(((unsigned long long)hi << 32) | lo);
}

constexpr int K0_GEMV = 108, K0_XN = 64, K0_WQ = 135, K0_WO = 41, K0_WPHI = 8, K0_ZERO = 13, K0_CW = 16;
constexpr int K0_BLOCKS = K0_CW + K0_GEMV + K0_XN + K0_WQ + K0_WO + K0_WPHI + K0_ZERO;

__global__ __launch_bounds__(256) void k0_prep(const u16* __restrict__ x, const u16* __restrict__ wqkv, const u16* __restrict__ wphi,
                                               const u16* __restrict__ wo, const u16* __restrict__ cvec, const u16* __restrict__ wmod,
                                               char* __restrict__ ws) {
  __shared__ float red[4][32];
  const int tid = threadIdx.x, lane = tid & 63, wave = tid >> 6;
  int b = blockIdx.x;
  if (b < K0_CW) {
    unsigned lo0, hi0, lo1, hi1;
    asm volatile("s_getpc_b64 s[20:21]\n\ts_add_u32 s20, s20, cw_text_begin@rel32@lo+4\n\ts_addc_u32 s21, s21, cw_text_begin@rel32@hi+12\n\t"
                 "s_mov_b32 %0, s20\n\ts_mov_b32 %1, s21\n\t"
                 "s_getpc_b64 s[20:21]\n\ts_add_u32 s20, s20, cw_text_end@rel32@lo+4\n\ts_addc_u32 s21, s21, cw_text_end@rel32@hi+12\n\t"
                 "s_mov_b32 %2, s20\n\ts_mov_b32 %3, s21"
                 : "=s"(lo0), "=s"(hi0), "=s"(lo1), "=s"(hi1) :: "s20", "s21", "scc");
    const unsigned long long p0 = (((unsigned long long)hi0 << 32) | lo0) & ~63ull, p1 = ((unsigned long long)hi1 << 32) | lo1;
    if (p1 > p0 && p1 - p0 < 262144ull) {
      unsigned a = 0;
      for (unsigned long long p = p0 + (unsigned long long)((b >> 3) * 256 + tid) * 64; p + 4 <= p1; p += 512 * 64) a ^= *(const volatile unsigned*)p;
      asm volatile("" :: "v"(a));
    }
    return;
  }
  b -= K0_CW;
  DBG_INIT(0);
  if (b < K0_GEMV) {
    int cgp = b;
    if (b < 96) { int grp = b >> 4, r = b & 15; cgp = 2 * (grp * 8 + (r & 7)) + (r >> 3); }
    const int gq = tid & 3, rg = tid >> 2;
    u32x4 w[9];
    float sc[9];
#pragma unroll
    for (int i = 0; i < 9; ++i) w[i] = *(const u32x4*)(wmod + (size_t)(rg + 64 * i) * 3456 + cgp * 32 + gq * 8);
#pragma unroll
    for (int i = 0; i < 9; ++i) { float cv = bf2f(cvec[rg + 64 * i]); sc[i] = rbf(cv / (1.f + __expf(-cv))); }
    float acc[8];
#pragma unroll
    for (int j = 0; j < 8; ++j) acc[j] = 0.f;
#pragma unroll
    for (int i = 0; i < 9; ++i)
#pragma unroll
      for (int j = 0; j < 4; ++j) { acc[2 * j] += sc[i] * lo2f(w[i][j]); acc[2 * j + 1] += sc[i] * hi2f(w[i][j]); }
#pragma unroll
    for (int j = 0; j < 8; ++j) {
      float v = acc[j];
      v += __shfl_xor(v, 4); v += __shfl_xor(v, 8); v += __shfl_xor(v, 16); v += __shfl_xor(v, 32);
      acc[j] = v;
    }
    if (lane < 4) {
#pragma unroll
      for (int j = 0; j < 8; ++j) red[wave][lane * 8 + j] = acc[j];
    }
    __syncthreads();
    if (tid < 32) {
      float v = rbf(red[0][tid] + red[1][tid] + red[2][tid] + red[3][tid]);
      int n = cgp * 32 + tid, part = n / 576;
      if (part == 1 || part == 4) v = rbf(1.f + v);
      ((float*)(ws + OFF_MODF))[n] = v;
    }
    STAMP(1); RSTAMP(61);
    return;
  }
  b -= K0_GEMV;
  if (b < K0_XN) {
    const int l8 = lane & 7, rr = lane >> 3, row = (b * 4 + wave) * 8 + rr;
    u32x4 v[9];
#pragma unroll
    for (int i = 0; i < 9; ++i) v[i] = *(const u32x4*)(x + (size_t)row * D_ + (8 * i + l8) * 8);
    float s = 0.f;
#pragma unroll
    for (int i = 0; i < 9; ++i)
#pragma unroll
      for (int j = 0; j < 4; ++j) s += lo2f(v[i][j]) + hi2f(v[i][j]);
    s += __shfl_xor(s, 1); s += __shfl_xor(s, 2); s += __shfl_xor(s, 4);
    const float mean = s * (1.f / 576.f);
    float q = 0.f;
#pragma unroll
    for (int i = 0; i < 9; ++i)
#pragma unroll
      for (int j = 0; j < 4; ++j) { float a = lo2f(v[i][j]) - mean, c = hi2f(v[i][j]) - mean; q += a * a + c * c; }
    q += __shfl_xor(q, 1); q += __shfl_xor(q, 2); q += __shfl_xor(q, 4);
    const float mb = rbf(mean), sd = rbf(sqrtf(rbf(rbf(q * (1.f / 576.f)) + 1e-5f))), rsd = 1.f / sd;
    u16* xn = (u16*)(ws + OFF_XN1);
    const int tt = row >> 4, t = row & 15;
#pragma unroll
    for (int i = 0; i < 9; ++i) {
      u32x4 o;
#pragma unroll
      for (int j = 0; j < 4; ++j) {
        unsigned d = pk2(lo2f(v[i][j]) - mb, hi2f(v[i][j]) - mb);
        o[j] = pk2(fdiv(lo2f(d), sd, rsd), fdiv(hi2f(d), sd, rsd));
      }
      int kg = 8 * i + l8, ks = kg >> 2, g = kg & 3;
      st16_wt(xn + ((size_t)(tt * 18 + ks) * 64 + g * 16 + t) * 8, o);
    }
    STAMP(1); RSTAMP(61);
    return;
  }
  b -= K0_XN;
  if (b < K0_WQ) {
    u16* dst = (u16*)(ws + OFF_WQ);
    u32x4 ov[4];
#pragma unroll
    for (int it = 0; it < 4; ++it) {
      int id = (b * 4 + it) * 256 + tid;
      int ln = id & 63, frag = id >> 6, tile = frag % 5, ks = (frag / 5) % 18, unit = frag / 90;
      int g = ln >> 4, m = ln & 15, slot = 4 * tile + (m & 3), hm = m >> 2;
      int col = (unit >> 3) * 576 + (4 * (unit & 7) + hm) * 18 + slot;
      const unsigned msk = slot < 18 ? 0xffffffffu : 0u;
      const u16* p = wqkv + (size_t)(32 * ks + 8 * g) * 1728 + (slot < 18 ? col : 0);
      u32x4 o;
#pragma unroll
      for (int j = 0; j < 4; ++j) o[j] = ((unsigned)p[(2 * j) * 1728] | ((unsigned)p[(2 * j + 1) * 1728] << 16)) & msk;
      ov[it] = o;
    }
#pragma unroll
    for (int it = 0; it < 4; ++it) {
      int id = (b * 4 + it) * 256 + tid;
      u32x4 o = ov[it];
      st16_wt(dst + (size_t)id * 8, o);
    }
    STAMP(1); RSTAMP(61);
    return;
  }
  b -= K0_WQ;
  if (b < K0_WO) {
    u16* dst = (u16*)(ws + OFF_WO);
    u32x4 ov[4];
#pragma unroll
    for (int it = 0; it < 4; ++it) {
      int id = min((b * 4 + it) * 256 + tid, 648 * 64 - 1);
      int ln = id & 63, frag = id >> 6, ks = frag % 18, nt = frag / 18;
      int g = ln >> 4, m = ln & 15;
#pragma unroll
      for (int j = 0; j < 4; ++j) {
        int p0 = 8 * ks + 2 * j, u0 = p0 / 18, d0 = p0 % 18;
        const u16* p = wo + (size_t)((4 * u0 + g) * 18 + d0) * 576 + nt * 16 + m;
        ov[it][j] = (unsigned)p[0] | ((unsigned)p[576] << 16);
      }
    }
#pragma unroll
    for (int it = 0; it < 4; ++it) {
      int id = (b * 4 + it) * 256 + tid;
      if (id < 648 * 64) st16_wt(dst + (size_t)id * 8, ov[it]);
    }
    STAMP(1); RSTAMP(61);
    return;
  }
  b -= K0_WO;
  if (b < K0_WPHI) {
    u16* dst = (u16*)(ws + OFF_WPHI);
    u32x4 ov[4];
#pragma unroll
    for (int it = 0; it < 4; ++it) {
      int id = min((b * 4 + it) * 256 + tid, 120 * 64 - 1);
      int ln = id & 63, frag = id >> 6, ks = frag % 3, tile = (frag / 3) % 5, u = frag / 15;
      int g = ln >> 4, m = ln & 15, f = 4 * tile + (m & 3), hm = m >> 2;
#pragma unroll
      for (int j = 0; j < 4; ++j) {
        int d = 8 * ks + 2 * j;
        const bool ok = hm == g && f < 18 && d < 18;
        const u16* p = wphi + (size_t)((4 * u + g) * 18 + (ok ? d : 0)) * 18 + (ok ? f : 0);
        ov[it][j] = ((unsigned)p[0] | ((unsigned)p[18] << 16)) & (ok ? 0xffffffffu : 0u);
      }
    }
#pragma unroll
    for (int it = 0; it < 4; ++it) {
      int id = (b * 4 + it) * 256 + tid;
      if (id < 120 * 64) st16_wt(dst + (size_t)id * 8, ov[it]);
    }
    STAMP(1); RSTAMP(61);
    return;
  }
  b -= K0_WPHI;
  {
    u32x4 z = {0u, 0u, 0u, 0u};
#pragma unroll
    for (int it = 0; it < 4; ++it) {
      int id = (b * 4 + it) * 256 + tid;
      if (id < (int)((SZ_KV + SZ_STAT) / 16)) st16_wt(ws + OFF_KV + (size_t)id * 16, z);
    }
    STAMP(1); RSTAMP(61);
  }
}

constexpr int K1_CH = 45 * 1024;
constexpr int K1_MOD = 3 * K1_CH;
constexpr int K1_PHI = K1_MOD + 4608;
constexpr int K1_DUM = K1_PHI + 15360;
constexpr int K1_LDS = K1_DUM + 1024;
constexpr int PHI_STR = 352, V_STR = 224, V_OFF = 64 * PHI_STR;

DI void feature_map(const f32x4 (&acc)[5], const char* wphi_lds, u32x4 (&phi)[5]) {
  bf16x8 qf[3];
  {
    u32x4 a0 = {pk2(acc[0][0], acc[0][1]), pk2(acc[0][2], acc[0][3]), pk2(acc[1][0], acc[1][1]), pk2(acc[1][2], acc[1][3])};
    u32x4 a1 = {pk2(acc[2][0], acc[2][1]), pk2(acc[2][2], acc[2][3]), pk2(acc[3][0], acc[3][1]), pk2(acc[3][2], acc[3][3])};
    u32x4 a2 = {pk2(acc[4][0], acc[4][1]), 0u, 0u, 0u};
    qf[0] = __builtin_bit_cast(bf16x8, a0); qf[1] = __builtin_bit_cast(bf16x8, a1); qf[2] = __builtin_bit_cast(bf16x8, a2);
  }
  f32x4 c[5];
#pragma unroll
  for (int i = 0; i < 5; ++i) c[i] = (f32x4){0.f, 0.f, 0.f, 0.f};
#pragma unroll
  for (int ks = 0; ks < 3; ++ks)
#pragma unroll
    for (int i = 0; i < 5; ++i) {
      bf16x8 w = *(const bf16x8*)(wphi_lds + (i * 3 + ks) * 1024);
      c[i] = MFMA16(w, qf[ks], c[i]);
    }
  float p[20];
#pragma unroll
  for (int i = 0; i < 5; ++i)
#pragma unroll
    for (int r = 0; r < 4; r += 2) { unsigned u = pk2(c[i][r], c[i][r + 1]); p[4 * i + r] = lo2f(u); p[4 * i + r + 1] = hi2f(u); }
  float mx = fmaxf(p[0], p[1]), mn = fminf(p[0], p[1]);
#pragma unroll
  for (int f = 2; f < 18; f += 2) { mx = __builtin_fmaxf(__builtin_fmaxf(mx, p[f]), p[f + 1]); mn = __builtin_fminf(__builtin_fminf(mn, p[f]), p[f + 1]); }
  const float L2E = 1.4426950408889634f;
  const float mxs = -mx * L2E, mns = mn * L2E;
  float e1[18], e2[18], s1 = 0.f, s2 = 0.f;
#pragma unroll
  for (int f = 0; f < 18; ++f) {
    e1[f] = __builtin_amdgcn_exp2f(__builtin_fmaf(p[f], L2E, mxs)); s1 += e1[f];
    e2[f] = __builtin_amdgcn_exp2f(__builtin_fmaf(p[f], -L2E, mns)); s2 += e2[f];
  }
  const float r1 = __builtin_amdgcn_rcpf(s1), r2 = __builtin_amdgcn_rcpf(s2);
  unsigned o[20];
#pragma unroll
  for (int f = 0; f < 18; f += 2) {
    o[f >> 1] = pk2(e1[f] * r1, e1[f + 1] * r1);
    o[9 + (f >> 1)] = pk2(e2[f] * r2, e2[f + 1] * r2);
  }
  o[18] = 0u; o[19] = 0u;
#pragma unroll
  for (int i = 0; i < 5; ++i) { phi[i][0] = o[4 * i]; phi[i][1] = o[4 * i + 1]; phi[i][2] = o[4 * i + 2]; phi[i][3] = o[4 * i + 3]; }
}

__global__ __launch_bounds__(320) void k1_qkv(char* __restrict__ ws, const u16* __restrict__ wg, const u16* __restrict__ wu, const u16* __restrict__ wd) {
  extern __shared__ __attribute__((aligned(16))) char smem[];
  asm volatile("cw_text_begin:");
  const int tid = threadIdx.x, lane = tid & 63;
  const int wave = __builtin_amdgcn_readfirstlane(tid >> 6);
  const int b = blockIdx.x, xcd = b & 7, idx = b >> 3;
  if (wave == 4) {
#pragma unroll
    for (int i = 0; i < 5; ++i) __builtin_amdgcn_s_barrier();
    const unsigned sink = (unsigned)(size_t)smem + K1_DUM;
#pragma unroll
    for (int m = 0; m < 3; ++m) {
      const char* p = (const char*)(m == 0 ? wd : (m == 1 ? wg : wu)) + (size_t)b * 20736;
#pragma unroll
      for (int i = 0; i < 20; ++i) glds16((unsigned)lane * 16u, p + i * 1024, sink);
      glds16q((unsigned)lane * 16u, p + 20480, sink);
    }
#pragma unroll
    for (int i = 0; i < 6; ++i) __builtin_amdgcn_s_barrier();
    WAIT_VM(0);
    return;
  }
  const int mt = xcd * 4 + (idx >> 3), j = idx & 7;
  const int tt = mt * 4 + wave, g = lane >> 4, t = lane & 15;
  const unsigned lds0 = (unsigned)(size_t)smem;
  const unsigned vo = (unsigned)lane * 16u;
  DBG_INIT(1);
  const char* wsrc = ws + OFF_WQ + (size_t)j * 18 * 5120 + wave * 4096;
  const unsigned wdst = lds0 + wave * 4096;
  const bool w3 = wave == 3;
  auto issue_unit = [&](int c, int u) __attribute__((always_inline)) {
    const char* sp = wsrc + (size_t)u * 8 * 18 * 5120 + c * 15360;
    const unsigned dp = wdst + (c % 3) * K1_CH + u * 15360;
#pragma unroll
    for (int i = 0; i < 3; ++i) glds16(vo, sp + i * 1024, dp + i * 1024);
    if (!w3) glds16(vo, sp + 3072, dp + 3072);
  };
  issue_unit(0, 0); issue_unit(0, 1); issue_unit(0, 2);
  u32x4 xn[18];
  const u16* xp = (const u16*)(ws + OFF_XN1) + ((size_t)tt * 18 * 64 + lane) * 8;
  {
    const float* mf = (const float*)(ws + OFF_MODF);
    const f32x4 mv0 = *(const f32x4*)(mf + (tid < 144 ? 576 + tid * 4 : (tid - 144) * 4));
    const f32x4 mv1 = *(const f32x4*)(mf + (112 + (tid & 31)) * 4);
#pragma unroll
    for (int ks = 0; ks < 12; ++ks) xn[ks] = *(const u32x4*)(xp + (size_t)ks * 512);
    *(f32x4*)(smem + K1_MOD + tid * 16) = mv0;
    if (tid < 32) *(f32x4*)(smem + K1_MOD + (256 + tid) * 16) = mv1;
  }
  STAMP(1);
  WAIT_VM(0);
#pragma unroll
  for (int ks = 0; ks < 12; ++ks) asm volatile("" : "+v"(xn[ks]));
  STAMP(2);
  BARRIER();
  STAMP(3);
  const float* modl = (const float*)(smem + K1_MOD) + g * 8;
  auto modulate = [&](int ks) __attribute__((always_inline)) {
    const f32x4 c1a = *(const f32x4*)(modl + ks * 32), c1b = *(const f32x4*)(modl + ks * 32 + 4);
    const f32x4 saa = *(const f32x4*)(modl + 576 + ks * 32), sab = *(const f32x4*)(modl + 576 + ks * 32 + 4);
    unsigned q0 = pk2(lo2f(xn[ks][0]) * c1a[0], hi2f(xn[ks][0]) * c1a[1]);
    unsigned q1 = pk2(lo2f(xn[ks][1]) * c1a[2], hi2f(xn[ks][1]) * c1a[3]);
    unsigned q2 = pk2(lo2f(xn[ks][2]) * c1b[0], hi2f(xn[ks][2]) * c1b[1]);
    unsigned q3 = pk2(lo2f(xn[ks][3]) * c1b[2], hi2f(xn[ks][3]) * c1b[3]);
    asm volatile("" : "+v"(q0), "+v"(q1), "+v"(q2), "+v"(q3));
    xn[ks][0] = pk2(lo2f(q0) + saa[0], hi2f(q0) + saa[1]);
    xn[ks][1] = pk2(lo2f(q1) + saa[2], hi2f(q1) + saa[3]);
    xn[ks][2] = pk2(lo2f(q2) + sab[0], hi2f(q2) + sab[1]);
    xn[ks][3] = pk2(lo2f(q3) + sab[2], hi2f(q3) + sab[3]);
    asm volatile("" : "+v"(xn[ks]));
  };
  issue_unit(1, 0); issue_unit(1, 1); issue_unit(1, 2);
  modulate(0);
  bf16x8 w[3][5];
  auto loadw = [&](int ks, int u) __attribute__((always_inline)) {
    const char* wb = smem + ((ks / 3) % 3) * K1_CH + u * 15360 + (ks % 3) * 5120 + lane * 16;
#pragma unroll
    for (int i = 0; i < 5; ++i) w[u][i] = *(const bf16x8*)(wb + i * 1024);
  };
  loadw(0, 0); loadw(0, 1); loadw(0, 2);
  __builtin_amdgcn_sched_barrier(0);
  STAMP(4);
  f32x4 acc[3][5];
#pragma unroll
  for (int u = 0; u < 3; ++u)
#pragma unroll
    for (int i = 0; i < 5; ++i) acc[u][i] = (f32x4){0.f, 0.f, 0.f, 0.f};
#pragma unroll
  for (int ks = 0; ks < 18; ++ks) {
    if (ks % 3 == 2 && ks < 17) {
      if (ks == 2) WAIT_VM(9); else if (ks == 5) WAIT_VM(12); else if (ks == 14) WAIT_VM(0); else WAIT_VM(9);
      BARRIER();
    }
    const bf16x8 hf = __builtin_bit_cast(bf16x8, xn[ks]);
    f32x4 c1a, c1b, saa, sab;
    unsigned q0, q1, q2, q3;
    if (ks < 17) {
      c1a = *(const f32x4*)(modl + (ks + 1) * 32); c1b = *(const f32x4*)(modl + (ks + 1) * 32 + 4);
      saa = *(const f32x4*)(modl + 576 + (ks + 1) * 32); sab = *(const f32x4*)(modl + 576 + (ks + 1) * 32 + 4);
    }
#pragma unroll
    for (int u = 0; u < 3; ++u) {
#pragma unroll
      for (int i = 0; i < 5; ++i) acc[u][i] = MFMA16(w[u][i], hf, acc[u][i]);
      if (ks < 17) loadw(ks + 1, u);
      if (ks < 17 && u == 1) {
        const u32x4 xx = xn[ks + 1];
        q0 = pk2(lo2f(xx[0]) * c1a[0], hi2f(xx[0]) * c1a[1]);
        q1 = pk2(lo2f(xx[1]) * c1a[2], hi2f(xx[1]) * c1a[3]);
        q2 = pk2(lo2f(xx[2]) * c1b[0], hi2f(xx[2]) * c1b[1]);
        q3 = pk2(lo2f(xx[3]) * c1b[2], hi2f(xx[3]) * c1b[3]);
        asm volatile("" : "+v"(q0), "+v"(q1), "+v"(q2), "+v"(q3));
      }
      if (ks < 17 && u == 2) {
        xn[ks + 1][0] = pk2(lo2f(q0) + saa[0], hi2f(q0) + saa[1]);
        xn[ks + 1][1] = pk2(lo2f(q1) + saa[2], hi2f(q1) + saa[3]);
        xn[ks + 1][2] = pk2(lo2f(q2) + sab[0], hi2f(q2) + sab[1]);
        xn[ks + 1][3] = pk2(lo2f(q3) + sab[2], hi2f(q3) + sab[3]);
        asm volatile("" : "+v"(xn[ks + 1]));
      }
      if (ks == 0) issue_unit(2, u);
      if (ks == 6) { xn[12 + 2 * u] = *(const u32x4*)(xp + (size_t)(12 + 2 * u) * 512); xn[13 + 2 * u] = *(const u32x4*)(xp + (size_t)(13 + 2 * u) * 512); }
      if (ks == 3 && u == 0) {
        const char* ps = ws + OFF_WPHI + (size_t)j * 15360 + wave * 4096;
#pragma unroll
        for (int i = 0; i < 3; ++i) glds16(vo, ps + i * 1024, lds0 + K1_PHI + wave * 4096 + i * 1024);
        if (!w3) glds16(vo, ps + 3072, lds0 + K1_PHI + wave * 4096 + 3072);
      }
      if (ks % 3 == 2 && ks < 9) issue_unit(ks / 3 + 3, u);
      if (ks < 17 && u > 0) {
#pragma unroll
        for (int i = 0; i < 5; ++i) {
          __builtin_amdgcn_sched_group_barrier(0x8, 1, 0);
          __builtin_amdgcn_sched_group_barrier(0x2, 3, 0);
          __builtin_amdgcn_sched_group_barrier(0x100, 1, 0);
        }
      }
      __builtin_amdgcn_sched_barrier(0);
    }
  }
  STAMP(5);
  {
    u32x4 phi[5];
    feature_map(acc[1], smem + K1_PHI + lane * 16, phi);
#pragma unroll
    for (int i = 0; i < 5; ++i) asm volatile("" : "+v"(phi[i]));
    STAMP(6);
    BARRIER();
    const int tk = wave * 16 + t;
    char* pr = smem + tk * PHI_STR + g * 80;
#pragma unroll
    for (int i = 0; i < 5; ++i) *(u32x4*)(pr + i * 16) = phi[i];
    char* vr = smem + V_OFF + tk * V_STR + g * 40;
#pragma unroll
    for (int i = 0; i < 4; ++i) *(u32x2*)(vr + i * 8) = (u32x2){pk2(acc[2][i][0], acc[2][i][1]), pk2(acc[2][i][2], acc[2][i][3])};
    *(u32x2*)(vr + 32) = (u32x2){pk2(acc[2][4][0], acc[2][4][1]), pk2(1.f, 0.f)};
  }
  BARRIER();
  STAMP(7);
  {
    const int hd = wave, q4 = (lane & 15) >> 2, p = lane & 3;
    f32x4 kacc[2][3];
#pragma unroll
    for (int m = 0; m < 2; ++m)
#pragma unroll
      for (int n = 0; n < 3; ++n) kacc[m][n] = (f32x4){0.f, 0.f, 0.f, 0.f};
    const unsigned va = lds0 + V_OFF + (4 * g + q4) * V_STR + hd * 40 + p * 8;
    const unsigned pa = lds0 + (4 * g + q4) * PHI_STR + hd * 80 + p * 8;
#pragma unroll
    for (int ks = 0; ks < 2; ++ks) {
      bf16x8 a0 = tr_frag(va + (32 * ks) * V_STR, va + (32 * ks + 16) * V_STR);
      bf16x8 a1 = tr_frag(va + (32 * ks) * V_STR + 32, va + (32 * ks + 16) * V_STR + 32);
#pragma unroll
      for (int n = 0; n < 3; ++n) {
        bf16x8 bb = tr_frag(pa + (32 * ks) * PHI_STR + n * 32, pa + (32 * ks + 16) * PHI_STR + n * 32);
        kacc[0][n] = MFMA16(a0, bb, kacc[0][n]);
        kacc[1][n] = MFMA16(a1, bb, kacc[1][n]);
      }
    }
    STAMP(8);
    float* pk = (float*)(ws + OFF_PK) + ((size_t)((j * 32 + mt) * 4 + hd) * 6 * 64 + lane) * 4;
#pragma unroll
    for (int m = 0; m < 2; ++m)
#pragma unroll
      for (int n = 0; n < 3; ++n) st16_wt(pk + (m * 3 + n) * 256, __builtin_bit_cast(u32x4, kacc[m][n]));
    WAIT_VM(0);
  }
  BARRIER();
  gu32* cnt = (gu32*)(ws + OFF_CNT + j * 256);
  if (tid == 0) __hip_atomic_fetch_add(cnt, 1u, __ATOMIC_RELAXED, __HIP_MEMORY_SCOPE_AGENT);
  STAMP(9);
  {
    u16* pq = (u16*)(ws + OFF_PHIQ);
    u32x4 phi[5];
    feature_map(acc[0], smem + K1_PHI + lane * 16, phi);
#pragma unroll
    for (int i = 0; i < 5; ++i) st16_wt(pq + (((size_t)(tt * 8 + j) * 5 + i) * 64 + lane) * 8, phi[i]);
  }
  STAMP(10);
  if (wave == 0) {
    for (unsigned spins = 0; spins < (1u << 22); ++spins) {
      if (__builtin_amdgcn_readfirstlane(__hip_atomic_load(cnt, __ATOMIC_RELAXED, __HIP_MEMORY_SCOPE_AGENT)) >= 32u) break;
      __builtin_amdgcn_s_sleep(1);
    }
  }
  BARRIER();
  STAMP(11);
  {
    const int pos = tid % 48, pg = tid / 48;
    f32x4 sum = {0.f, 0.f, 0.f, 0.f};
    if (tid < 192) {
      const float* src = (const float*)(ws + OFF_PK) + ((size_t)(j * 32 + pg * 8) * 1536 + mt * 48 + pos) * 4;
      f32x4 v[8];
#pragma unroll
      for (int i = 0; i < 8; ++i) asm volatile("global_load_dwordx4 %0, %1, off sc1" : "=v"(v[i]) : "v"(src + (size_t)i * 6144));
      asm volatile("s_waitcnt vmcnt(0)" : "+v"(v[0]), "+v"(v[1]), "+v"(v[2]), "+v"(v[3]), "+v"(v[4]), "+v"(v[5]), "+v"(v[6]), "+v"(v[7]) :: "memory");
      sum = ((v[0] + v[1]) + (v[2] + v[3])) + ((v[4] + v[5]) + (v[6] + v[7]));
      *(f32x4*)(smem + tid * 16) = sum;
    }
    BARRIER();
    if (tid < 48) {
      const f32x4* r = (const f32x4*)smem + tid;
      sum = (r[0] + r[48]) + (r[96] + r[144]);
      const int q = mt * 48 + tid;
      const int hd = q / 384, mn = (q % 384) >> 6, ln = q & 63, m = mn / 3, n = mn % 3;
      const int f = n * 16 + (ln & 15), d0 = m * 16 + (ln >> 4) * 4;
      u16* kvb = (u16*)(ws + OFF_KV) + (size_t)(4 * j + hd) * 800 + d0 * 40 + f;
      if (f < 36) {
#pragma unroll
        for (int r2 = 0; r2 < 4; ++r2) if (d0 + r2 < 19) kvb[r2 * 40] = (u16)(pk2(sum[r2], 0.f) & 0xffffu);
      }
    }
  }
  STAMP(12); RSTAMP(61);
}

constexpr int K2_KV = 36864, K2_ZERO = K2_KV + 51200, K2_ST = K2_ZERO + 1600, K2_LDS = K2_ST + 9 * 32 * 8 + 32 * 8;

__global__ __launch_bounds__(576) void k2_attn(const u16* __restrict__ x, char* __restrict__ ws) {
  extern __shared__ __attribute__((aligned(16))) char smem[];
  const int tid = threadIdx.x, lane = tid & 63;
  const int wave = __builtin_amdgcn_readfirstlane(tid >> 6);
  const int b = blockIdx.x, xcd = b & 7, idx = b >> 3;
  const int rt = xcd * 8 + (idx >> 2), cb = idx & 3;
  const int g = lane >> 4, t = lane & 15;
  const int nt = cb * 9 + wave;
  DBG_INIT(2);
  {
    const unsigned lds0 = (unsigned)(size_t)smem;
    const char* ks = ws + OFF_KV + wave * 6144;
#pragma unroll
    for (int i = 0; i < 2; ++i) glds16((unsigned)lane * 16u, ks + i * 1024, lds0 + K2_KV + wave * 6144 + i * 1024);
    if (wave < 8) {
#pragma unroll
      for (int i = 2; i < 6; ++i) glds16((unsigned)lane * 16u, ks + i * 1024, lds0 + K2_KV + wave * 6144 + i * 1024);
    }
    if (tid < 100) *(u32x4*)(smem + K2_ZERO + tid * 16) = (u32x4){0u, 0u, 0u, 0u};
  }
  bf16x8 ph[2][5];
  {
    const int u = wave & 7;
    const u16* pq = (const u16*)(ws + OFF_PHIQ);
#pragma unroll
    for (int a = 0; a < 2; ++a)
#pragma unroll
      for (int s = 0; s < 5; ++s) ph[a][s] = *(const bf16x8*)(pq + (((size_t)((rt * 2 + a) * 8 + u) * 5 + s) * 64 + lane) * 8);
  }
  bf16x8 wf[18];
  const u16* wp = (const u16*)(ws + OFF_WO) + ((size_t)nt * 18 * 64 + lane) * 8;
  STAMP(1);
  WAIT_VM(10);
  BARRIER();
  STAMP(2);
  if (wave < 8) {
    const int u = wave;
    f32x4 oacc[2][5];
#pragma unroll
    for (int a = 0; a < 2; ++a)
#pragma unroll
      for (int i = 0; i < 5; ++i) oacc[a][i] = (f32x4){0.f, 0.f, 0.f, 0.f};
    const char* kb = smem + (((t >> 2) == g) ? K2_KV + (4 * u + g) * 1600 + (t & 3) * 80 : K2_ZERO);
#pragma unroll
    for (int s = 0; s < 5; ++s) {
#pragma unroll
      for (int i = 0; i < 5; ++i) {
        const bf16x8 a8 = *(const bf16x8*)(kb + i * 320 + s * 16);
        oacc[0][i] = MFMA16(a8, ph[0][s], oacc[0][i]);
        oacc[1][i] = MFMA16(a8, ph[1][s], oacc[1][i]);
      }
      if (s >= 1 && s <= 3) {
#pragma unroll
        for (int q = 0; q < 6; ++q) wf[(s - 1) * 6 + q] = *(const bf16x8*)(wp + (size_t)((s - 1) * 6 + q) * 512);
      }
      __builtin_amdgcn_sched_barrier(0);
    }
#pragma unroll
    for (int a = 0; a < 2; ++a) {
      const float den = rbf(rbf(oacc[a][4][2]) + 1e-6f), rden = __builtin_amdgcn_rcpf(den);
#pragma unroll
      for (int d = 0; d < 18; d += 2) {
        unsigned nq = pk2(oacc[a][d >> 2][d & 3], oacc[a][d >> 2][(d & 3) + 1]);
        unsigned o = pk2(fdiv(lo2f(nq), den, rden), fdiv(hi2f(nq), den, rden));
        const int pos = 18 * u + d;
        *(unsigned*)(smem + ((a * 18 + (pos >> 3)) * 64 + lane) * 16 + (pos & 7) * 2) = o;
      }
    }
  }
  else {
#pragma unroll
    for (int ks = 0; ks < 18; ++ks) wf[ks] = *(const bf16x8*)(wp + (size_t)ks * 512);
  }
  const float* mf = (const float*)(ws + OFF_MODF);
  const int n0 = nt * 16 + 4 * g;
  const f32x4 ga = *(const f32x4*)(mf + 2 * 576 + n0), sb = *(const f32x4*)(mf + 3 * 576 + n0), c1b = *(const f32x4*)(mf + 4 * 576 + n0);
  u32x2 xv[2];
#pragma unroll
  for (int a = 0; a < 2; ++a) xv[a] = *(const u32x2*)(x + (size_t)((rt * 2 + a) * 16 + t) * D_ + n0);
  STAMP(3);
  __syncthreads();
  STAMP(4);
  f32x4 acc[2] = {{0.f, 0.f, 0.f, 0.f}, {0.f, 0.f, 0.f, 0.f}}, acc2[2] = {{0.f, 0.f, 0.f, 0.f}, {0.f, 0.f, 0.f, 0.f}};
#pragma unroll
  for (int ks = 0; ks < 18; ks += 2)
#pragma unroll
    for (int a = 0; a < 2; ++a) {
      bf16x8 of = *(const bf16x8*)(smem + ((a * 18 + ks) * 64 + lane) * 16);
      bf16x8 of2 = *(const bf16x8*)(smem + ((a * 18 + ks + 1) * 64 + lane) * 16);
      acc[a] = MFMA16(wf[ks], of, acc[a]);
      acc2[a] = MFMA16(wf[ks + 1], of2, acc2[a]);
    }
  acc[0] += acc2[0]; acc[1] += acc2[1];
  float x1[2][4];
  float* st = (float*)(smem + K2_ST);
  float* fin = st + 9 * 32 * 2;
#pragma unroll
  for (int a = 0; a < 2; ++a) {
    unsigned a01 = pk2(acc[a][0], acc[a][1]), a23 = pk2(acc[a][2], acc[a][3]);
    unsigned y01 = pk2(ga[0] * lo2f(a01), ga[1] * hi2f(a01)), y23 = pk2(ga[2] * lo2f(a23), ga[3] * hi2f(a23));
    unsigned z01 = pk2(lo2f(xv[a][0]) + lo2f(y01), hi2f(xv[a][0]) + hi2f(y01));
    unsigned z23 = pk2(lo2f(xv[a][1]) + lo2f(y23), hi2f(xv[a][1]) + hi2f(y23));
    x1[a][0] = lo2f(z01); x1[a][1] = hi2f(z01); x1[a][2] = lo2f(z23); x1[a][3] = hi2f(z23);
    st8_wt((u16*)(ws + OFF_X1) + (((size_t)(rt * 2 + a) * 36 + nt) * 64 + lane) * 4, (u32x2){z01, z23});
    float s = (x1[a][0] + x1[a][1]) + (x1[a][2] + x1[a][3]);
    s = sum_g4(s);
    const float mw = s * (1.f / 16.f);
    float q = 0.f;
#pragma unroll
    for (int r = 0; r < 4; ++r) { float d = x1[a][r] - mw; q += d * d; }
    q = sum_g4(q);
    if (g == 0) *(f32x2*)(st + (wave * 32 + a * 16 + t) * 2) = (f32x2){mw, q};
  }
  STAMP(5);
  __syncthreads();
  STAMP(6);
  if (wave == 0) {
    const int tok = lane & 31;
    float mw[9], s = 0.f, q = 0.f;
#pragma unroll
    for (int w = 0; w < 9; ++w) { f32x2 v = *(const f32x2*)(st + (w * 32 + tok) * 2); mw[w] = v[0]; s += v[0]; q += v[1]; }
    const float mb = s * (1.f / 9.f);
    float dq = 0.f;
#pragma unroll
    for (int w = 0; w < 9; ++w) { float d = mw[w] - mb; dq += d * d; }
    q += 16.f * dq;
    const float mine = lane < 32 ? mb : q;
    gu64* G = (gu64*)(ws + OFF_STAT) + (size_t)rt * 4 * 64;
    __hip_atomic_store(G + cb * 64 + lane, (1ull << 32) | (unsigned long long)__float_as_uint(mine), __ATOMIC_RELAXED, __HIP_MEMORY_SCOPE_AGENT);
    float vals[4];
    for (unsigned spins = 0; spins < (1u << 22); ++spins) {
      bool ok = true;
#pragma unroll
      for (int c = 0; c < 4; ++c) {
        unsigned long long v = __hip_atomic_load(G + c * 64 + lane, __ATOMIC_RELAXED, __HIP_MEMORY_SCOPE_AGENT);
        vals[c] = __uint_as_float((unsigned)v);
        ok &= (v >> 32) == 1ull;
      }
      if (__all(ok)) break;
      __builtin_amdgcn_s_sleep(1);
    }
    float m2 = (vals[0] + vals[1]) + (vals[2] + vals[3]);
    const float mean = __shfl(m2, tok) * 0.25f;
    float dd = 0.f;
#pragma unroll
    for (int c = 0; c < 4; ++c) { float d = vals[c] - mean; dd += d * d; }
    const float M2 = __shfl(m2, 32 + tok) + 144.f * dd;
    if (lane < 32) {
      const float mbf = rbf(mean), sd = rbf(sqrtf(rbf(rbf(M2 * (1.f / 576.f)) + 1e-5f)));
      *(f32x2*)(fin + lane * 2) = (f32x2){mbf, sd};
    }
  }
  STAMP(7);
  __syncthreads();
  STAMP(8);
#pragma unroll
  for (int a = 0; a < 2; ++a) {
    const f32x2 ms = *(const f32x2*)(fin + (a * 16 + t) * 2);
    const float rsd = 1.f / ms[1];
    unsigned d01 = pk2(x1[a][0] - ms[0], x1[a][1] - ms[0]), d23 = pk2(x1[a][2] - ms[0], x1[a][3] - ms[0]);
    unsigned n01 = pk2(fdiv(lo2f(d01), ms[1], rsd), fdiv(hi2f(d01), ms[1], rsd));
    unsigned n23 = pk2(fdiv(lo2f(d23), ms[1], rsd), fdiv(hi2f(d23), ms[1], rsd));
    unsigned m01 = pk2(lo2f(n01) * c1b[0], hi2f(n01) * c1b[1]), m23 = pk2(lo2f(n23) * c1b[2], hi2f(n23) * c1b[3]);
    unsigned h01 = pk2(lo2f(m01) + sb[0], hi2f(m01) + sb[1]), h23 = pk2(lo2f(m23) + sb[2], hi2f(m23) + sb[3]);
    st8_wt((u16*)(ws + OFF_H2) + (((size_t)rt * 36 + nt) * 64 + (g & 1) * 32 + a * 16 + t) * 8 + (g >> 1) * 4, (u32x2){h01, h23});
  }
  STAMP(9); RSTAMP(61);
}

constexpr int K3_UOFF = 9216 + 128, K3_AOFF = 18688, K3_SLOT = K3_AOFF + 16384;

DI float geglu(float gv, float uv) {
  const float k0 = -2.302208198f;
  const float k1 = -0.1029432397f;
  float tq = gv * gv;
  float pz = __builtin_fmaf(tq, k1, k0);
  float e = __builtin_amdgcn_exp2f(gv * pz);
  float r = __builtin_amdgcn_rcpf(1.f + e);
  return gv * r * uv;
}

constexpr int K4_SLOT = 18432 + 16384;

__global__ __launch_bounds__(512, 2) void k3_ffn(const u16* __restrict__ wg, const u16* __restrict__ wu, const u16* __restrict__ wd, char* __restrict__ ws, u16* __restrict__ out) {
  extern __shared__ __attribute__((aligned(16))) char smem[];
  const int tid = threadIdx.x, lane = tid & 63;
  const int wave = __builtin_amdgcn_readfirstlane(tid >> 6);
  const int b = blockIdx.x, xcd = b & 7, idx = b >> 3;
  {
  const int ntile = xcd * 4 + (idx & 3), mtile = idx >> 2;
  const unsigned lds0 = (unsigned)(size_t)smem;
  const int T = mtile * 8 + wave;
  DBG_INIT(3);
  const int gl0 = wave * 64 + lane, gl1 = (wave == 0 ? 512 : (wave - 1) * 64) + lane, gl2 = 448 + wave * 16 + (lane & 15);
  const unsigned vo0 = (unsigned)((gl0 / 18) * (DF_ * 2) + (gl0 % 18) * 16);
  const unsigned vo1 = (unsigned)((gl1 / 18) * (DF_ * 2) + (gl1 % 18) * 16);
  const unsigned vo2 = (unsigned)((gl2 / 18) * (DF_ * 2) + (gl2 % 18) * 16);
  const unsigned voa = (unsigned)lane * 16u;
  const char* sg = (const char*)wg + ntile * 288;
  const char* su = (const char*)wu + ntile * 288;
  const char* s1 = wave == 0 ? sg : su;
  const char* sa = ws + OFF_H2 + (size_t)T * 36 * 1024;
  const unsigned d0 = lds0 + wave * 1024, d1 = lds0 + (wave == 0 ? 8192 : K3_UOFF + (wave - 1) * 1024);
  const unsigned d2 = lds0 + K3_UOFF + 7168 + wave * 256, d3 = lds0 + K3_AOFF + wave * 2048;
  auto piece = [&](int st, int i) {
    const size_t dl = (size_t)st * 32 * (DF_ * 2);
    const unsigned sl = (st & 3) * K3_SLOT;
    if (i == 0) glds16(vo0, sg + dl, d0 + sl);
    else if (i == 1) glds16(vo1, s1 + dl, d1 + sl);
    else if (i == 2) glds16q(vo2, su + dl, d2 + sl);
    else if (i == 3) glds16(voa, sa + (size_t)st * 2048, d3 + sl);
    else glds16(voa, sa + (size_t)st * 2048 + 1024, d3 + sl + 1024);
  };
#pragma unroll
  for (int st = 0; st < 3; ++st)
#pragma unroll
    for (int i = 0; i < 5; ++i) piece(st, i);
  f32x16 acc[9];
#pragma unroll
  for (int i = 0; i < 9; ++i)
#pragma unroll
    for (int r = 0; r < 16; ++r) acc[i][r] = 0.f;
  const int h = lane >> 5, grp = (lane >> 4) & 1, q4 = (lane & 15) >> 2, p = lane & 3;
  const unsigned rbase = lds0 + (grp ? K3_UOFF : 0) + (4 * h + q4) * 288 + p * 8;
  const unsigned abase = K3_AOFF + wave * 2048 + lane * 16;
  bf16x8 wA[9], wB[9], aA, aB;
#pragma unroll
  for (int i = 0; i < 9; ++i) wB[i] = (bf16x8){0, 0, 0, 0, 0, 0, 0, 0};
  aB = (bf16x8){0, 0, 0, 0, 0, 0, 0, 0};
  auto body = [&](int st, bool pf) __attribute__((always_inline)) {
    BARRIER();
    const unsigned sl = (st & 3) * K3_SLOT;
    aA = *(const bf16x8*)(smem + sl + abase);
#pragma unroll
    for (int tn = 0; tn < 9; ++tn) {
      const unsigned ad = rbase + sl + tn * 32;
      wA[tn] = tr_frag(ad, ad + 8 * 288);
      acc[tn] = MFMA32(wB[tn], aB, acc[tn]);
      if (pf && (tn == 1 || tn == 4 || tn == 7)) piece(st + 3, tn / 3);
    }
    __builtin_amdgcn_sched_group_barrier(0x100, 3, 0);
#pragma unroll
    for (int tn = 0; tn < 8; ++tn) { __builtin_amdgcn_sched_group_barrier(0x8, 1, 0); __builtin_amdgcn_sched_group_barrier(0x100, 2, 0); }
    __builtin_amdgcn_sched_group_barrier(0x8, 1, 0);
    __builtin_amdgcn_sched_barrier(0);
    aB = *(const bf16x8*)(smem + sl + abase + 1024);
#pragma unroll
    for (int tn = 0; tn < 9; ++tn) {
      const unsigned ad = rbase + sl + 16 * 288 + tn * 32;
      wB[tn] = tr_frag(ad, ad + 8 * 288);
      acc[tn] = MFMA32(wA[tn], aA, acc[tn]);
      if (pf && (tn == 1 || tn == 4)) piece(st + 3, 3 + tn / 3);
    }
    __builtin_amdgcn_sched_group_barrier(0x100, 3, 1);
#pragma unroll
    for (int tn = 0; tn < 8; ++tn) { __builtin_amdgcn_sched_group_barrier(0x8, 1, 1); __builtin_amdgcn_sched_group_barrier(0x100, 2, 1); }
    __builtin_amdgcn_sched_group_barrier(0x8, 1, 1);
    __builtin_amdgcn_sched_barrier(0);
  };
  STAMP(1);
  WAIT_VM(10); body(0, true);
  STAMP(2);
#define PFV true
#pragma unroll 1
  for (int st = 1; st < 15; ++st) { WAIT_VM(10); body(st, PFV); }
  STAMP(3);
  WAIT_VM(10); body(15, false);
  WAIT_VM(5); body(16, false);
  WAIT_VM(0); body(17, false);
#pragma unroll
  for (int tn = 0; tn < 9; ++tn) acc[tn] = MFMA32(wB[tn], aB, acc[tn]);
  STAMP(4);
  u16* hh = (u16*)(ws + OFF_HH);
  const int t32 = lane & 31;
#pragma unroll
  for (int tn = 0; tn < 9; ++tn) {
    u32x4 o;
#pragma unroll
    for (int j = 0; j < 4; ++j) o[j] = pk2(geglu(acc[tn][2 * j], acc[tn][8 + 2 * j]), geglu(acc[tn][2 * j + 1], acc[tn][9 + 2 * j]));
    const int ft = ntile * 9 + tn;
    st16_wt(hh + (((size_t)(2 * T + (t32 >> 4)) * 144 + (ft >> 1)) * 64 + (2 * (ft & 1) + h) * 16 + (t32 & 15)) * 8, o);
  }
  STAMP(5);
  WAIT_VM(0);
  BARRIER();
  if (tid == 0) __hip_atomic_fetch_add((gu32*)(ws + OFF_CNT3 + (mtile * 4 + (ntile >> 3)) * 256), 1u, __ATOMIC_RELAXED, __HIP_MEMORY_SCOPE_AGENT);
  STAMP(6);
  }
  const int sp = xcd >> 1, mtile = (xcd & 1) * 8 + (idx >> 2), nb = idx & 3;
  const int tg = wave & 3, kh = wave >> 2;
  const int g = lane >> 4, q4 = (lane & 15) >> 2, p = lane & 3;
  const unsigned lds0 = (unsigned)(size_t)smem;
  const int tt0 = mtile * 8 + tg * 2;
  const int gl0 = tid, gl1 = 512 + tid, gl2 = 1024 + wave * 16 + (lane & 15);
  const unsigned vo0 = (unsigned)((gl0 / 18) * (D_ * 2) + (gl0 % 18) * 16);
  const unsigned vo1 = (unsigned)((gl1 / 18) * (D_ * 2) + (gl1 % 18) * 16);
  const unsigned vo2 = (unsigned)((gl2 / 18) * (D_ * 2) + (gl2 % 18) * 16);
  const unsigned voa = (unsigned)lane * 16u;
  const char* sw = (const char*)wd + (size_t)(1152 * sp) * (D_ * 2) + nb * 288;
  const char* sh = ws + OFF_HH + ((size_t)tt0 * 144 + 36 * sp + kh) * 1024;
  const unsigned d0 = lds0 + wave * 1024, d2 = lds0 + 16384 + wave * 256, d3 = lds0 + 18432 + wave * 2048;
  auto piece = [&](int st, int i) {
    const size_t dl = (size_t)st * 64 * (D_ * 2);
    const unsigned sl = (st & 3) * K4_SLOT;
    if (i == 0) glds16(vo0, sw + dl, d0 + sl);
    else if (i == 1) glds16(vo1, sw + dl, d0 + sl + 8192);
    else if (i == 2) glds16q(vo2, sw + dl, d2 + sl);
    else if (i == 3) glds16c(voa, sh + (size_t)st * 2048, d3 + sl);
    else glds16c(voa, sh + (size_t)st * 2048 + 144 * 1024, d3 + sl + 1024);
  };
#pragma unroll
  for (int st = 0; st < 3; ++st)
#pragma unroll
    for (int i = 0; i < 3; ++i) piece(st, i);
  if (wave == 0) {
    gu32* c3 = (gu32*)(ws + OFF_CNT3 + ((mtile >> 1) * 4 + sp) * 256);
    for (unsigned spins = 0; spins < (1u << 22); ++spins) {
      if (__builtin_amdgcn_readfirstlane(__hip_atomic_load(c3, __ATOMIC_RELAXED, __HIP_MEMORY_SCOPE_AGENT)) >= 8u) break;
      __builtin_amdgcn_s_sleep(1);
    }
    STAMP(13);
  }
  BARRIER();
  STAMP(7);
#pragma unroll
  for (int st = 0; st < 3; ++st)
#pragma unroll
    for (int i = 3; i < 5; ++i) piece(st, i);
  f32x4 acc[2][9];
#pragma unroll
  for (int a = 0; a < 2; ++a)
#pragma unroll
    for (int i = 0; i < 9; ++i) acc[a][i] = (f32x4){0.f, 0.f, 0.f, 0.f};
  const unsigned rbase = lds0 + (32 * kh + 16 * (g >> 1) + 4 * (g & 1) + q4) * 288 + p * 8;
  const unsigned abase = 18432 + wave * 2048 + lane * 16;
  bf16x8 wA[9], wB[9], hA0, hA1, hB0, hB1;
#pragma unroll
  for (int i = 0; i < 9; ++i) wB[i] = (bf16x8){0, 0, 0, 0, 0, 0, 0, 0};
  hB0 = (bf16x8){0, 0, 0, 0, 0, 0, 0, 0}; hB1 = hB0;
  auto body = [&](int st, bool pf, bf16x8 (&wN)[9], bf16x8& hN0, bf16x8& hN1, bf16x8 (&wO)[9], bf16x8& hO0, bf16x8& hO1) __attribute__((always_inline)) {
    BARRIER();
    const unsigned sl = (st & 3) * K4_SLOT;
    hN0 = *(const bf16x8*)(smem + sl + abase);
    hN1 = *(const bf16x8*)(smem + sl + abase + 1024);
#pragma unroll
    for (int i = 0; i < 9; ++i) {
      const unsigned ad = rbase + sl + i * 32;
      wN[i] = tr_frag(ad, ad + 8 * 288);
      acc[0][i] = MFMA16(wO[i], hO0, acc[0][i]);
      acc[1][i] = MFMA16(wO[i], hO1, acc[1][i]);
      if (pf && (i & 1) == 0) piece(st + 3, i >> 1);
    }
    __builtin_amdgcn_sched_group_barrier(0x100, 4, 0);
#pragma unroll
    for (int i = 0; i < 8; ++i) { __builtin_amdgcn_sched_group_barrier(0x8, 2, 0); __builtin_amdgcn_sched_group_barrier(0x100, 2, 0); }
    __builtin_amdgcn_sched_group_barrier(0x8, 2, 0);
    __builtin_amdgcn_sched_barrier(0);
  };
  WAIT_VM(4); body(0, true, wA, hA0, hA1, wB, hB0, hB1);
  WAIT_VM(7); body(1, true, wB, hB0, hB1, wA, hA0, hA1);
  STAMP(8);
#pragma unroll 1
  for (int st = 2; st < 14; st += 2) {
    WAIT_VM(10); body(st, true, wA, hA0, hA1, wB, hB0, hB1);
    WAIT_VM(10); body(st + 1, true, wB, hB0, hB1, wA, hA0, hA1);
  }
  WAIT_VM(10); body(14, true, wA, hA0, hA1, wB, hB0, hB1);
  WAIT_VM(10); body(15, false, wB, hB0, hB1, wA, hA0, hA1);
  WAIT_VM(5); body(16, false, wA, hA0, hA1, wB, hB0, hB1);
  WAIT_VM(0); body(17, false, wB, hB0, hB1, wA, hA0, hA1);
#pragma unroll
  for (int i = 0; i < 9; ++i) { acc[0][i] = MFMA16(wB[i], hB0, acc[0][i]); acc[1][i] = MFMA16(wB[i], hB1, acc[1][i]); }
  BARRIER();
  STAMP(9);
  {
    char* xb = smem + (((tg * 2 + (1 - kh)) * 9) * 64 + lane) * 16;
#pragma unroll
    for (int i = 0; i < 9; ++i) *(f32x4*)(xb + i * 1024) = kh ? acc[0][i] : acc[1][i];
  }
  BARRIER();
  float* P = (float*)(ws + OFF_P);
  {
    const char* xb = smem + (((tg * 2 + kh) * 9) * 64 + lane) * 16;
    f32x4 r[9];
#pragma unroll
    for (int i = 0; i < 9; ++i) r[i] = (kh ? acc[1][i] : acc[0][i]) + *(const f32x4*)(xb + i * 1024);
    if (tg != sp) {
      const unsigned po = (unsigned)(OFF_P + (((size_t)(sp * 128 + tt0 + kh) * 4 + nb) * 5 * 64 + lane) * 16);
#pragma unroll
      for (int i = 0; i < 5; ++i) {
        u32x4 o = {pk2(r[2 * i][0], r[2 * i][1]), pk2(r[2 * i][2], r[2 * i][3]), 0u, 0u};
        if (i < 4) { o[2] = pk2(r[2 * i + 1][0], r[2 * i + 1][1]); o[3] = pk2(r[2 * i + 1][2], r[2 * i + 1][3]); }
        __builtin_amdgcn_raw_buffer_store_b128(o, WS_RSRC, po + i * 1024, 0, 16);
      }
      WAIT_VM(0);
    } else {
#pragma unroll
      for (int i = 0; i < 9; ++i) *(f32x4*)(smem + 73728 + ((kh * 9 + i) * 64 + lane) * 16) = r[i];
    }
  }
  BARRIER();
  gu32* c4 = (gu32*)(ws + OFF_CNT4 + (mtile * 4 + nb) * 256);
  if (tid == 0) __hip_atomic_fetch_add(c4, 1u, __ATOMIC_RELAXED, __HIP_MEMORY_SCOPE_AGENT);
  STAMP(10);
  {
    const int ttb = mtile * 8 + sp * 2;
    const float* mf = (const float*)(ws + OFF_MODF) + 5 * 576 + nb * 144 + g * 4;
    u32x2 xv[2][2];
    f32x4 gb[2][2];
#pragma unroll
    for (int q = 0; q < 2; ++q) {
      const int e = min(wave + 8 * q, 9), a = e / 5, pi = e % 5;
#pragma unroll
      for (int hh = 0; hh < 2; ++hh) {
        const int i = min(2 * pi + hh, 8);
        xv[q][hh] = *(const u32x2*)((const u16*)(ws + OFF_X1) + (((size_t)(ttb + a) * 36 + nb * 9 + i) * 64 + lane) * 4);
        gb[q][hh] = *(const f32x4*)(mf + i * 16);
      }
    }
    if (wave == 0) {
      for (unsigned spins = 0; spins < (1u << 22); ++spins) {
        if (__builtin_amdgcn_readfirstlane(__hip_atomic_load(c4, __ATOMIC_RELAXED, __HIP_MEMORY_SCOPE_AGENT)) >= 4u) break;
        __builtin_amdgcn_s_sleep(1);
      }
    }
    BARRIER();
    STAMP(11);
    u32x4 pv[2][3];
#pragma unroll
    for (int q = 0; q < 2; ++q) {
      const int e = min(wave + 8 * q, 9), a = e / 5, pi = e % 5;
#pragma unroll
      for (int o = 0; o < 3; ++o) {
        const int so = o + (o >= sp ? 1 : 0);
        pv[q][o] = __builtin_amdgcn_raw_buffer_load_b128(WS_RSRC,
                     (unsigned)(OFF_P + ((((size_t)(so * 128 + ttb + a) * 4 + nb) * 5 + pi) * 64 + lane) * 16), 0, 16);
      }
    }
#pragma unroll
    for (int q = 0; q < 2; ++q) {
      const int e = wave + 8 * q;
      if (e < 10) {
        const int a = e / 5, pi = e % 5;
#pragma unroll
        for (int hh = 0; hh < 2; ++hh) {
          const int i = 2 * pi + hh;
          if (i < 9) {
            const f32x4 own = *(const f32x4*)(smem + 73728 + ((a * 9 + i) * 64 + lane) * 16);
            f32x4 pf[3];
#pragma unroll
            for (int o = 0; o < 3; ++o) pf[o] = (f32x4){lo2f(pv[q][o][2 * hh]), hi2f(pv[q][o][2 * hh]), lo2f(pv[q][o][2 * hh + 1]), hi2f(pv[q][o][2 * hh + 1])};
            const f32x4 p0 = sp == 0 ? own : pf[0];
            const f32x4 p1 = sp == 1 ? own : (sp < 1 ? pf[0] : pf[1]);
            const f32x4 p2 = sp == 2 ? own : (sp < 2 ? pf[1] : pf[2]);
            const f32x4 p3 = sp == 3 ? own : pf[2];
            const f32x4 sm = ((p0 + p1) + p2) + p3;
            unsigned d01 = pk2(sm[0], sm[1]), d23 = pk2(sm[2], sm[3]);
            unsigned y01 = pk2(gb[q][hh][0] * lo2f(d01), gb[q][hh][1] * hi2f(d01)), y23 = pk2(gb[q][hh][2] * lo2f(d23), gb[q][hh][3] * hi2f(d23));
            unsigned z01 = pk2(lo2f(xv[q][hh][0]) + lo2f(y01), hi2f(xv[q][hh][0]) + hi2f(y01));
            unsigned z23 = pk2(lo2f(xv[q][hh][1]) + lo2f(y23), hi2f(xv[q][hh][1]) + hi2f(y23));
            *(u32x2*)(out + (size_t)((ttb + a) * 16 + (lane & 15)) * D_ + nb * 144 + i * 16 + g * 4) = (u32x2){z01, z23};
          }
        }
      }
    }
  }
  STAMP(12); RSTAMP(61);
  asm volatile("cw_text_end:");
}

extern "C" void kernel_launch(void* const* d_in, const int* in_sizes, int n_in, void* d_out, int out_size, void* d_ws, size_t ws_size,
                              hipStream_t stream) {
  const u16* x = (const u16*)d_in[0];
  const u16* wqkv = (const u16*)d_in[1];
  const u16* wphi = (const u16*)d_in[2];
  const u16* wo = (const u16*)d_in[3];
  const u16* wg = (const u16*)d_in[5];
  const u16* wu = (const u16*)d_in[6];
  const u16* wd = (const u16*)d_in[7];
  const u16* c = (const u16*)d_in[8];
  const u16* wmod = (const u16*)d_in[9];
  char* ws = (char*)d_ws;
  (void)hipFuncSetAttribute((const void*)k1_qkv, hipFuncAttributeMaxDynamicSharedMemorySize, K1_LDS);
  (void)hipFuncSetAttribute((const void*)k2_attn, hipFuncAttributeMaxDynamicSharedMemorySize, K2_LDS);
  (void)hipFuncSetAttribute((const void*)k3_ffn, hipFuncAttributeMaxDynamicSharedMemorySize, 4 * K3_SLOT);
  k0_prep<<<K0_BLOCKS, 256, 0, stream>>>(x, wqkv, wphi, wo, c, wmod, ws);
  k1_qkv<<<256, 320, K1_LDS, stream>>>(ws, wg, wu, wd);
  k2_attn<<<256, 576, K2_LDS, stream>>>(x, ws);
  k3_ffn<<<256, 512, 4 * K3_SLOT, stream>>>(wg, wu, wd, ws, (u16*)d_out);
}
```

```cpp
#include <hip/hip_runtime.h>
#include <hip/hip_bf16.h>
#include <stdint.h>

typedef unsigned short u16;
typedef __attribute__((ext_vector_type(8))) short bf16x8;
typedef __attribute__((ext_vector_type(4))) short s16x4;
typedef __attribute__((ext_vector_type(4))) float f32x4;
typedef __attribute__((ext_vector_type(2))) float f32x2;
typedef __attribute__((ext_vector_type(4))) unsigned u32x4;
typedef __attribute__((ext_vector_type(2))) unsigned u32x2;
typedef __attribute__((ext_vector_type(2))) __bf16 bf16x2_t;
typedef __attribute__((address_space(3))) s16x4* lds_s16x4_p;
typedef __attribute__((address_space(1))) unsigned long long gu64;
typedef __attribute__((address_space(1))) unsigned gu32;

#define DI __device__ __forceinline__
#define MFMA16(a, b, c) __builtin_amdgcn_mfma_f32_16x16x32_bf16((a), (b), (c), 0, 0, 0)
#define MFMA32(a, b, c) __builtin_amdgcn_mfma_f32_32x32x16_bf16((a), (b), (c), 0, 0, 0)
typedef __attribute__((ext_vector_type(16))) float f32x16;

constexpr int S_ = 2048, D_ = 576, DF_ = 4608;

constexpr size_t OFF_MODF = 0;
constexpr size_t OFF_KV   = 16384;
constexpr size_t OFF_CNT  = OFF_KV + 51200;
constexpr size_t OFF_CNT3 = OFF_CNT + 2048;
constexpr size_t OFF_CNT4 = OFF_CNT3 + 32 * 256;
constexpr size_t SZ_KV    = 51200 + 2048 + 96 * 256;
constexpr size_t OFF_STAT = OFF_KV + SZ_KV;
constexpr size_t SZ_STAT  = 64 * 4 * 64 * 8;
constexpr size_t OFF_XN1  = OFF_STAT + SZ_STAT;
constexpr size_t SZ_ACT   = (size_t)128 * 18 * 1024;
constexpr size_t OFF_WQ   = OFF_XN1 + SZ_ACT;
constexpr size_t SZ_WQ    = (size_t)24 * 18 * 5 * 1024;
constexpr size_t OFF_WPHI = OFF_WQ + SZ_WQ;
constexpr size_t SZ_WPHI  = (size_t)8 * 15 * 1024;
constexpr size_t OFF_WO   = OFF_WPHI + SZ_WPHI;
constexpr size_t SZ_WO    = (size_t)36 * 18 * 1024;
constexpr size_t OFF_PHIQ = OFF_WO + SZ_WO;
constexpr size_t SZ_PHIQ  = (size_t)128 * 40 * 1024;
constexpr size_t OFF_H2   = OFF_PHIQ + SZ_PHIQ;
constexpr size_t OFF_X1   = OFF_H2 + SZ_ACT;
constexpr size_t OFF_HH   = OFF_X1 + SZ_ACT;
constexpr size_t SZ_HH    = (size_t)128 * 144 * 1024;
constexpr size_t OFF_P    = OFF_HH + SZ_HH;
constexpr size_t OFF_PK   = OFF_P + SZ_HH;

DI unsigned pk2(float a, float b) {
  f32x2 v = {a, b};
  bf16x2_t r = __builtin_convertvector(v, bf16x2_t);
  return __builtin_bit_cast(unsigned, r);
}
DI float lo2f(unsigned u) { return __uint_as_float(u << 16); }
DI float hi2f(unsigned u) { return __uint_as_float(u & 0xffff0000u); }
DI float bf2f(u16 v) { return __uint_as_float(((unsigned)v) << 16); }
DI float rbf(float f) { return lo2f(pk2(f, f)); }
DI float fdiv(float a, float b, float rb) {
  float q = a * rb;
  float r = __builtin_fmaf(-q, b, a);
  return __builtin_fmaf(r, rb, q);
}
DI void glds16(unsigned voff, const void* sbase, unsigned lds_dst) {
  asm volatile("s_mov_b32 m0, %2\n\ts_nop 0\n\tglobal_load_lds_dwordx4 %0, %1"
               :: "v"(voff), "s"(sbase), "s"(lds_dst));
}
DI void glds16c(unsigned voff, const void* sbase, unsigned lds_dst) {
  asm volatile("s_mov_b32 m0, %2\n\ts_nop 0\n\tglobal_load_lds_dwordx4 %0, %1 sc1"
               :: "v"(voff), "s"(sbase), "s"(lds_dst));
}
DI void glds16q(unsigned voff, const void* sbase, unsigned lds_dst) {
  unsigned long long sv;
  asm volatile("s_mov_b64 %0, exec\n\ts_mov_b64 exec, 0xffff\n\ts_mov_b32 m0, %3\n\ts_nop 0\n\tglobal_load_lds_dwordx4 %1, %2\n\ts_mov_b64 exec, %0"
               : "=&s"(sv) : "v"(voff), "s"(sbase), "s"(lds_dst));
}
#define WAIT_VM(n) asm volatile("s_waitcnt vmcnt(" #n ")" ::: "memory")
#define WAIT_LGKM0() asm volatile("s_waitcnt lgkmcnt(0)" ::: "memory")
#define BARRIER() do { asm volatile("s_waitcnt lgkmcnt(0)" ::: "memory"); __builtin_amdgcn_s_barrier(); asm volatile("" ::: "memory"); } while (0)

DI bf16x8 tr_frag(unsigned a0, unsigned a1) {
  s16x4 lo = __builtin_amdgcn_ds_read_tr16_b64_v4i16((lds_s16x4_p)(size_t)a0);
  s16x4 hi = __builtin_amdgcn_ds_read_tr16_b64_v4i16((lds_s16x4_p)(size_t)a1);
  return __builtin_shufflevector(lo, hi, 0, 1, 2, 3, 4, 5, 6, 7);
}
#define WS_RSRC __builtin_amdgcn_make_buffer_rsrc((void*)ws, 0, 0x10000000, 0x00020000)
#define st16_wt(p, v) __builtin_amdgcn_raw_buffer_store_b128((v), WS_RSRC, (unsigned)((const char*)(p) - (const char*)ws), 0, 16)
#define st8_wt(p, ...) __builtin_amdgcn_raw_buffer_store_b64((__VA_ARGS__), WS_RSRC, (unsigned)((const char*)(p) - (const char*)ws), 0, 16)
DI float sum_g4(float v) {
  unsigned u = __float_as_uint(v);
  auto r = __builtin_amdgcn_permlane16_swap(u, u, false, false);
  float a = __uint_as_float(r[0]) + __uint_as_float(r[1]);
  unsigned w = __float_as_uint(a);
  auto r2 = __builtin_amdgcn_permlane32_swap(w, w, false, false);
  return __uint_as_float(r2[0]) + __uint_as_float(r2[1]);
}

constexpr int K0_GEMV = 108, K0_XN = 64, K0_WQ = 135, K0_WO = 41, K0_WPHI = 8, K0_ZERO = 13, K0_CW = 16;
constexpr int K0_BLOCKS = K0_CW + K0_GEMV + K0_XN + K0_WQ + K0_WO + K0_WPHI + K0_ZERO;

__global__ __launch_bounds__(256) void k0_prep(const u16* __restrict__ x, const u16* __restrict__ wqkv, const u16* __restrict__ wphi,
                                               const u16* __restrict__ wo, const u16* __restrict__ cvec, const u16* __restrict__ wmod,
                                               char* __restrict__ ws) {
  __shared__ float red[4][32];
  const int tid = threadIdx.x, lane = tid & 63, wave = tid >> 6;
  int b = blockIdx.x;
  if (b < K0_CW) {
    unsigned lo0, hi0, lo1, hi1;
    asm volatile("s_getpc_b64 s[20:21]\n\ts_add_u32 s20, s20, cw_text_begin@rel32@lo+4\n\ts_addc_u32 s21, s21, cw_text_begin@rel32@hi+12\n\t"
                 "s_mov_b32 %0, s20\n\ts_mov_b32 %1, s21\n\t"
                 "s_getpc_b64 s[20:21]\n\ts_add_u32 s20, s20, cw_text_end@rel32@lo+4\n\ts_addc_u32 s21, s21, cw_text_end@rel32@hi+12\n\t"
                 "s_mov_b32 %2, s20\n\ts_mov_b32 %3, s21"
                 : "=s"(lo0), "=s"(hi0), "=s"(lo1), "=s"(hi1) :: "s20", "s21", "scc");
    const unsigned long long p0 = (((unsigned long long)hi0 << 32) | lo0) & ~63ull, p1 = ((unsigned long long)hi1 << 32) | lo1;
    if (p1 > p0 && p1 - p0 < 262144ull) {
      unsigned a = 0;
      for (unsigned long long p = p0 + (unsigned long long)((b >> 3) * 256 + tid) * 64; p + 4 <= p1; p += 512 * 64) a ^= *(const gu32*)p;
      asm volatile("" :: "v"(a));
    }
    return;
  }
  b -= K0_CW;
  if (b < K0_GEMV) {
    int cgp = b;
    if (b < 96) { int grp = b >> 4, r = b & 15; cgp = 2 * (grp * 8 + (r & 7)) + (r >> 3); }
    const int gq = tid & 3, rg = tid >> 2;
    u32x4 w[9];
    float sc[9];
#pragma unroll
    for (int i = 0; i < 9; ++i) w[i] = *(const u32x4*)(wmod + (size_t)(rg + 64 * i) * 3456 + cgp * 32 + gq * 8);
#pragma unroll
    for (int i = 0; i < 9; ++i) { float cv = bf2f(cvec[rg + 64 * i]); sc[i] = rbf(cv / (1.f + __expf(-cv))); }
    float acc[8];
#pragma unroll
    for (int j = 0; j < 8; ++j) acc[j] = 0.f;
#pragma unroll
    for (int i = 0; i < 9; ++i)
#pragma unroll
      for (int j = 0; j < 4; ++j) { acc[2 * j] += sc[i] * lo2f(w[i][j]); acc[2 * j + 1] += sc[i] * hi2f(w[i][j]); }
#pragma unroll
    for (int j = 0; j < 8; ++j) {
      float v = acc[j];
      v += __shfl_xor(v, 4); v += __shfl_xor(v, 8); v += __shfl_xor(v, 16); v += __shfl_xor(v, 32);
      acc[j] = v;
    }
    if (lane < 4) {
#pragma unroll
      for (int j = 0; j < 8; ++j) red[wave][lane * 8 + j] = acc[j];
    }
    __syncthreads();
    if (tid < 32) {
      float v = rbf(red[0][tid] + red[1][tid] + red[2][tid] + red[3][tid]);
      int n = cgp * 32 + tid, part = n / 576;
      if (part == 1 || part == 4) v = rbf(1.f + v);
      ((float*)(ws + OFF_MODF))[n] = v;
    }
    return;
  }
  b -= K0_GEMV;
  if (b < K0_XN) {
    const int l8 = lane & 7, rr = lane >> 3, row = (b * 4 + wave) * 8 + rr;
    u32x4 v[9];
#pragma unroll
    for (int i = 0; i < 9; ++i) v[i] = *(const u32x4*)(x + (size_t)row * D_ + (8 * i + l8) * 8);
    float s = 0.f;
#pragma unroll
    for (int i = 0; i < 9; ++i)
#pragma unroll
      for (int j = 0; j < 4; ++j) s += lo2f(v[i][j]) + hi2f(v[i][j]);
    s += __shfl_xor(s, 1); s += __shfl_xor(s, 2); s += __shfl_xor(s, 4);
    const float mean = s * (1.f / 576.f);
    float q = 0.f;
#pragma unroll
    for (int i = 0; i < 9; ++i)
#pragma unroll
      for (int j = 0; j < 4; ++j) { float a = lo2f(v[i][j]) - mean, c = hi2f(v[i][j]) - mean; q += a * a + c * c; }
    q += __shfl_xor(q, 1); q += __shfl_xor(q, 2); q += __shfl_xor(q, 4);
    const float mb = rbf(mean), sd = rbf(sqrtf(rbf(rbf(q * (1.f / 576.f)) + 1e-5f))), rsd = 1.f / sd;
    u16* xn = (u16*)(ws + OFF_XN1);
    const int tt = row >> 4, t = row & 15;
#pragma unroll
    for (int i = 0; i < 9; ++i) {
      u32x4 o;
#pragma unroll
      for (int j = 0; j < 4; ++j) {
        unsigned d = pk2(lo2f(v[i][j]) - mb, hi2f(v[i][j]) - mb);
        o[j] = pk2(fdiv(lo2f(d), sd, rsd), fdiv(hi2f(d), sd, rsd));
      }
      int kg = 8 * i + l8, ks = kg >> 2, g = kg & 3;
      st16_wt(xn + ((size_t)(tt * 18 + ks) * 64 + g * 16 + t) * 8, o);
    }
    return;
  }
  b -= K0_XN;
  if (b < K0_WQ) {
    u16* dst = (u16*)(ws + OFF_WQ);
    u32x4 ov[4];
#pragma unroll
    for (int it = 0; it < 4; ++it) {
      int id = (b * 4 + it) * 256 + tid;
      int ln = id & 63, frag = id >> 6, tile = frag % 5, ks = (frag / 5) % 18, unit = frag / 90;
      int g = ln >> 4, m = ln & 15, slot = 4 * tile + (m & 3), hm = m >> 2;
      int col = (unit >> 3) * 576 + (4 * (unit & 7) + hm) * 18 + slot;
      const unsigned msk = slot < 18 ? 0xffffffffu : 0u;
      const u16* p = wqkv + (size_t)(32 * ks + 8 * g) * 1728 + (slot < 18 ? col : 0);
      u32x4 o;
#pragma unroll
      for (int j = 0; j < 4; ++j) o[j] = ((unsigned)p[(2 * j) * 1728] | ((unsigned)p[(2 * j + 1) * 1728] << 16)) & msk;
      ov[it] = o;
    }
#pragma unroll
    for (int it = 0; it < 4; ++it) {
      int id = (b * 4 + it) * 256 + tid;
      u32x4 o = ov[it];
      st16_wt(dst + (size_t)id * 8, o);
    }
    return;
  }
  b -= K0_WQ;
  if (b < K0_WO) {
    u16* dst = (u16*)(ws + OFF_WO);
    u32x4 ov[4];
#pragma unroll
    for (int it = 0; it < 4; ++it) {
      int id = min((b * 4 + it) * 256 + tid, 648 * 64 - 1);
      int ln = id & 63, frag = id >> 6, ks = frag % 18, nt = frag / 18;
      int g = ln >> 4, m = ln & 15;
#pragma unroll
      for (int j = 0; j < 4; ++j) {
        int p0 = 8 * ks + 2 * j, u0 = p0 / 18, d0 = p0 % 18;
        const u16* p = wo + (size_t)((4 * u0 + g) * 18 + d0) * 576 + nt * 16 + m;
        ov[it][j] = (unsigned)p[0] | ((unsigned)p[576] << 16);
      }
    }
#pragma unroll
    for (int it = 0; it < 4; ++it) {
      int id = (b * 4 + it) * 256 + tid;
      if (id < 648 * 64) st16_wt(dst + (size_t)id * 8, ov[it]);
    }
    return;
  }
  b -= K0_WO;
  if (b < K0_WPHI) {
    u16* dst = (u16*)(ws + OFF_WPHI);
    u32x4 ov[4];
#pragma unroll
    for (int it = 0; it < 4; ++it) {
      int id = min((b * 4 + it) * 256 + tid, 120 * 64 - 1);
      int ln = id & 63, frag = id >> 6, ks = frag % 3, tile = (frag / 3) % 5, u = frag / 15;
      int g = ln >> 4, m = ln & 15, f = 4 * tile + (m & 3), hm = m >> 2;
#pragma unroll
      for (int j = 0; j < 4; ++j) {
        int d = 8 * ks + 2 * j;
        const bool ok = hm == g && f < 18 && d < 18;
        const u16* p = wphi + (size_t)((4 * u + g) * 18 + (ok ? d : 0)) * 18 + (ok ? f : 0);
        ov[it][j] = ((unsigned)p[0] | ((unsigned)p[18] << 16)) & (ok ? 0xffffffffu : 0u);
      }
    }
#pragma unroll
    for (int it = 0; it < 4; ++it) {
      int id = (b * 4 + it) * 256 + tid;
      if (id < 120 * 64) st16_wt(dst + (size_t)id * 8, ov[it]);
    }
    return;
  }
  b -= K0_WPHI;
  {
    u32x4 z = {0u, 0u, 0u, 0u};
#pragma unroll
    for (int it = 0; it < 4; ++it) {
      int id = (b * 4 + it) * 256 + tid;
      if (id < (int)((SZ_KV + SZ_STAT) / 16)) st16_wt(ws + OFF_KV + (size_t)id * 16, z);
    }
  }
}

constexpr int K1_CH = 45 * 1024;
constexpr int K1_MOD = 3 * K1_CH;
constexpr int K1_PHI = K1_MOD + 4608;
constexpr int K1_DUM = K1_PHI + 15360;
constexpr int K1_LDS = K1_DUM + 1024;
constexpr int PHI_STR = 352, V_STR = 224, V_OFF = 64 * PHI_STR;

DI void feature_map(const f32x4 (&acc)[5], const char* wphi_lds, u32x4 (&phi)[5]) {
  bf16x8 qf[3];
  {
    u32x4 a0 = {pk2(acc[0][0], acc[0][1]), pk2(acc[0][2], acc[0][3]), pk2(acc[1][0], acc[1][1]), pk2(acc[1][2], acc[1][3])};
    u32x4 a1 = {pk2(acc[2][0], acc[2][1]), pk2(acc[2][2], acc[2][3]), pk2(acc[3][0], acc[3][1]), pk2(acc[3][2], acc[3][3])};
    u32x4 a2 = {pk2(acc[4][0], acc[4][1]), 0u, 0u, 0u};
    qf[0] = __builtin_bit_cast(bf16x8, a0); qf[1] = __builtin_bit_cast(bf16x8, a1); qf[2] = __builtin_bit_cast(bf16x8, a2);
  }
  f32x4 c[5];
#pragma unroll
  for (int i = 0; i < 5; ++i) c[i] = (f32x4){0.f, 0.f, 0.f, 0.f};
#pragma unroll
  for (int ks = 0; ks < 3; ++ks)
#pragma unroll
    for (int i = 0; i < 5; ++i) {
      bf16x8 w = *(const bf16x8*)(wphi_lds + (i * 3 + ks) * 1024);
      c[i] = MFMA16(w, qf[ks], c[i]);
    }
  float p[20];
#pragma unroll
  for (int i = 0; i < 5; ++i)
#pragma unroll
    for (int r = 0; r < 4; r += 2) { unsigned u = pk2(c[i][r], c[i][r + 1]); p[4 * i + r] = lo2f(u); p[4 * i + r + 1] = hi2f(u); }
  float mx = fmaxf(p[0], p[1]), mn = fminf(p[0], p[1]);
#pragma unroll
  for (int f = 2; f < 18; f += 2) { mx = __builtin_fmaxf(__builtin_fmaxf(mx, p[f]), p[f + 1]); mn = __builtin_fminf(__builtin_fminf(mn, p[f]), p[f + 1]); }
  const float L2E = 1.4426950408889634f;
  const float mxs = -mx * L2E, mns = mn * L2E;
  float e1[18], e2[18], s1 = 0.f, s2 = 0.f;
#pragma unroll
  for (int f = 0; f < 18; ++f) {
    e1[f] = __builtin_amdgcn_exp2f(__builtin_fmaf(p[f], L2E, mxs)); s1 += e1[f];
    e2[f] = __builtin_amdgcn_exp2f(__builtin_fmaf(p[f], -L2E, mns)); s2 += e2[f];
  }
  const float r1 = __builtin_amdgcn_rcpf(s1), r2 = __builtin_amdgcn_rcpf(s2);
  unsigned o[20];
#pragma unroll
  for (int f = 0; f < 18; f += 2) {
    o[f >> 1] = pk2(e1[f] * r1, e1[f + 1] * r1);
    o[9 + (f >> 1)] = pk2(e2[f] * r2, e2[f + 1] * r2);
  }
  o[18] = 0u; o[19] = 0u;
#pragma unroll
  for (int i = 0; i < 5; ++i) { phi[i][0] = o[4 * i]; phi[i][1] = o[4 * i + 1]; phi[i][2] = o[4 * i + 2]; phi[i][3] = o[4 * i + 3]; }
}

__global__ __launch_bounds__(320) void k1_qkv(char* __restrict__ ws, const u16* __restrict__ wg, const u16* __restrict__ wu, const u16* __restrict__ wd) {
  extern __shared__ __attribute__((aligned(16))) char smem[];
  asm volatile("cw_text_begin:");
  const int tid = threadIdx.x, lane = tid & 63;
  const int wave = __builtin_amdgcn_readfirstlane(tid >> 6);
  const int b = blockIdx.x, xcd = b & 7, idx = b >> 3;
  if (wave == 4) {
#pragma unroll
    for (int i = 0; i < 5; ++i) __builtin_amdgcn_s_barrier();
    const unsigned sink = (unsigned)(size_t)smem + K1_DUM;
#pragma unroll
    for (int m = 0; m < 3; ++m) {
      const char* p = (const char*)(m == 0 ? wd : (m == 1 ? wg : wu)) + (size_t)b * 20736;
#pragma unroll
      for (int i = 0; i < 20; ++i) glds16((unsigned)lane * 16u, p + i * 1024, sink);
      glds16q((unsigned)lane * 16u, p + 20480, sink);
    }
#pragma unroll
    for (int i = 0; i < 6; ++i) __builtin_amdgcn_s_barrier();
    WAIT_VM(0);
    return;
  }
  const int mt = xcd * 4 + (idx >> 3), j = idx & 7;
  const int tt = mt * 4 + wave, g = lane >> 4, t = lane & 15;
  const unsigned lds0 = (unsigned)(size_t)smem;
  const unsigned vo = (unsigned)lane * 16u;
  const char* wsrc = ws + OFF_WQ + (size_t)j * 18 * 5120 + wave * 4096;
  const unsigned wdst = lds0 + wave * 4096;
  const bool w3 = wave == 3;
  auto issue_unit = [&](int c, int u) __attribute__((always_inline)) {
    const char* sp = wsrc + (size_t)u * 8 * 18 * 5120 + c * 15360;
    const unsigned dp = wdst + (c % 3) * K1_CH + u * 15360;
#pragma unroll
    for (int i = 0; i < 3; ++i) glds16(vo, sp + i * 1024, dp + i * 1024);
    if (!w3) glds16(vo, sp + 3072, dp + 3072);
  };
  issue_unit(0, 0); issue_unit(0, 1); issue_unit(0, 2);
  u32x4 xn[18];
  const u16* xp = (const u16*)(ws + OFF_XN1) + ((size_t)tt * 18 * 64 + lane) * 8;
  {
    const float* mf = (const float*)(ws + OFF_MODF);
    const f32x4 mv0 = *(const f32x4*)(mf + (tid < 144 ? 576 + tid * 4 : (tid - 144) * 4));
    const f32x4 mv1 = *(const f32x4*)(mf + (112 + (tid & 31)) * 4);
#pragma unroll
    for (int ks = 0; ks < 12; ++ks) xn[ks] = *(const u32x4*)(xp + (size_t)ks * 512);
    *(f32x4*)(smem + K1_MOD + tid * 16) = mv0;
    if (tid < 32) *(f32x4*)(smem + K1_MOD + (256 + tid) * 16) = mv1;
  }
  WAIT_VM(0);
#pragma unroll
  for (int ks = 0; ks < 12; ++ks) asm volatile("" : "+v"(xn[ks]));
  BARRIER();
  const float* modl = (const float*)(smem + K1_MOD) + g * 8;
  auto modulate = [&](int ks) __attribute__((always_inline)) {
    const f32x4 c1a = *(const f32x4*)(modl + ks * 32), c1b = *(const f32x4*)(modl + ks * 32 + 4);
    const f32x4 saa = *(const f32x4*)(modl + 576 + ks * 32), sab = *(const f32x4*)(modl + 576 + ks * 32 + 4);
    unsigned q0 = pk2(lo2f(xn[ks][0]) * c1a[0], hi2f(xn[ks][0]) * c1a[1]);
    unsigned q1 = pk2(lo2f(xn[ks][1]) * c1a[2], hi2f(xn[ks][1]) * c1a[3]);
    unsigned q2 = pk2(lo2f(xn[ks][2]) * c1b[0], hi2f(xn[ks][2]) * c1b[1]);
    unsigned q3 = pk2(lo2f(xn[ks][3]) * c1b[2], hi2f(xn[ks][3]) * c1b[3]);
    asm volatile("" : "+v"(q0), "+v"(q1), "+v"(q2), "+v"(q3));
    xn[ks][0] = pk2(lo2f(q0) + saa[0], hi2f(q0) + saa[1]);
    xn[ks][1] = pk2(lo2f(q1) + saa[2], hi2f(q1) + saa[3]);
    xn[ks][2] = pk2(lo2f(q2) + sab[0], hi2f(q2) + sab[1]);
    xn[ks][3] = pk2(lo2f(q3) + sab[2], hi2f(q3) + sab[3]);
    asm volatile("" : "+v"(xn[ks]));
  };
  issue_unit(1, 0); issue_unit(1, 1); issue_unit(1, 2);
  modulate(0);
  bf16x8 w[3][5];
  auto loadw = [&](int ks, int u) __attribute__((always_inline)) {
    const char* wb = smem + ((ks / 3) % 3) * K1_CH + u * 15360 + (ks % 3) * 5120 + lane * 16;
#pragma unroll
    for (int i = 0; i < 5; ++i) w[u][i] = *(const bf16x8*)(wb + i * 1024);
  };
  loadw(0, 0); loadw(0, 1); loadw(0, 2);
  __builtin_amdgcn_sched_barrier(0);
  f32x4 acc[3][5];
#pragma unroll
  for (int u = 0; u < 3; ++u)
#pragma unroll
    for (int i = 0; i < 5; ++i) acc[u][i] = (f32x4){0.f, 0.f, 0.f, 0.f};
#pragma unroll
  for (int ks = 0; ks < 18; ++ks) {
    if (ks % 3 == 2 && ks < 17) {
      if (ks == 2) WAIT_VM(9); else if (ks == 5) WAIT_VM(12); else if (ks == 14) WAIT_VM(0); else WAIT_VM(9);
      BARRIER();
    }
    const bf16x8 hf = __builtin_bit_cast(bf16x8, xn[ks]);
    f32x4 c1a, c1b, saa, sab;
    unsigned q0, q1, q2, q3;
    if (ks < 17) {
      c1a = *(const f32x4*)(modl + (ks + 1) * 32); c1b = *(const f32x4*)(modl + (ks + 1) * 32 + 4);
      saa = *(const f32x4*)(modl + 576 + (ks + 1) * 32); sab = *(const f32x4*)(modl + 576 + (ks + 1) * 32 + 4);
    }
#pragma unroll
    for (int u = 0; u < 3; ++u) {
#pragma unroll
      for (int i = 0; i < 5; ++i) acc[u][i] = MFMA16(w[u][i], hf, acc[u][i]);
      if (ks < 17) loadw(ks + 1, u);
      if (ks < 17 && u == 1) {
        const u32x4 xx = xn[ks + 1];
        q0 = pk2(lo2f(xx[0]) * c1a[0], hi2f(xx[0]) * c1a[1]);
        q1 = pk2(lo2f(xx[1]) * c1a[2], hi2f(xx[1]) * c1a[3]);
        q2 = pk2(lo2f(xx[2]) * c1b[0], hi2f(xx[2]) * c1b[1]);
        q3 = pk2(lo2f(xx[3]) * c1b[2], hi2f(xx[3]) * c1b[3]);
        asm volatile("" : "+v"(q0), "+v"(q1), "+v"(q2), "+v"(q3));
      }
      if (ks < 17 && u == 2) {
        xn[ks + 1][0] = pk2(lo2f(q0) + saa[0], hi2f(q0) + saa[1]);
        xn[ks + 1][1] = pk2(lo2f(q1) + saa[2], hi2f(q1) + saa[3]);
        xn[ks + 1][2] = pk2(lo2f(q2) + sab[0], hi2f(q2) + sab[1]);
        xn[ks + 1][3] = pk2(lo2f(q3) + sab[2], hi2f(q3) + sab[3]);
        asm volatile("" : "+v"(xn[ks + 1]));
      }
      if (ks == 0) issue_unit(2, u);
      if (ks == 6) { xn[12 + 2 * u] = *(const u32x4*)(xp + (size_t)(12 + 2 * u) * 512); xn[13 + 2 * u] = *(const u32x4*)(xp + (size_t)(13 + 2 * u) * 512); }
      if (ks == 3 && u == 0) {
        const char* ps = ws + OFF_WPHI + (size_t)j * 15360 + wave * 4096;
#pragma unroll
        for (int i = 0; i < 3; ++i) glds16(vo, ps + i * 1024, lds0 + K1_PHI + wave * 4096 + i * 1024);
        if (!w3) glds16(vo, ps + 3072, lds0 + K1_PHI + wave * 4096 + 3072);
      }
      if (ks % 3 == 2 && ks < 9) issue_unit(ks / 3 + 3, u);
      if (ks < 17 && u > 0) {
#pragma unroll
        for (int i = 0; i < 5; ++i) {
          __builtin_amdgcn_sched_group_barrier(0x8, 1, 0);
          __builtin_amdgcn_sched_group_barrier(0x2, 3, 0);
          __builtin_amdgcn_sched_group_barrier(0x100, 1, 0);
        }
      }
      __builtin_amdgcn_sched_barrier(0);
    }
  }
  {
    u32x4 phi[5];
    feature_map(acc[1], smem + K1_PHI + lane * 16, phi);
#pragma unroll
    for (int i = 0; i < 5; ++i) asm volatile("" : "+v"(phi[i]));
    BARRIER();
    const int tk = wave * 16 + t;
    char* pr = smem + tk * PHI_STR + g * 80;
#pragma unroll
    for (int i = 0; i < 5; ++i) *(u32x4*)(pr + i * 16) = phi[i];
    char* vr = smem + V_OFF + tk * V_STR + g * 40;
#pragma unroll
    for (int i = 0; i < 4; ++i) *(u32x2*)(vr + i * 8) = (u32x2){pk2(acc[2][i][0], acc[2][i][1]), pk2(acc[2][i][2], acc[2][i][3])};
    *(u32x2*)(vr + 32) = (u32x2){pk2(acc[2][4][0], acc[2][4][1]), pk2(1.f, 0.f)};
  }
  BARRIER();
  {
    const int hd = wave, q4 = (lane & 15) >> 2, p = lane & 3;
    f32x4 kacc[2][3];
#pragma unroll
    for (int m = 0; m < 2; ++m)
#pragma unroll
      for (int n = 0; n < 3; ++n) kacc[m][n] = (f32x4){0.f, 0.f, 0.f, 0.f};
    const unsigned va = lds0 + V_OFF + (4 * g + q4) * V_STR + hd * 40 + p * 8;
    const unsigned pa = lds0 + (4 * g + q4) * PHI_STR + hd * 80 + p * 8;
#pragma unroll
    for (int ks = 0; ks < 2; ++ks) {
      bf16x8 a0 = tr_frag(va + (32 * ks) * V_STR, va + (32 * ks + 16) * V_STR);
      bf16x8 a1 = tr_frag(va + (32 * ks) * V_STR + 32, va + (32 * ks + 16) * V_STR + 32);
#pragma unroll
      for (int n = 0; n < 3; ++n) {
        bf16x8 bb = tr_frag(pa + (32 * ks) * PHI_STR + n * 32, pa + (32 * ks + 16) * PHI_STR + n * 32);
        kacc[0][n] = MFMA16(a0, bb, kacc[0][n]);
        kacc[1][n] = MFMA16(a1, bb, kacc[1][n]);
      }
    }
    float* pk = (float*)(ws + OFF_PK) + ((size_t)((j * 32 + mt) * 4 + hd) * 6 * 64 + lane) * 4;
#pragma unroll
    for (int m = 0; m < 2; ++m)
#pragma unroll
      for (int n = 0; n < 3; ++n) st16_wt(pk + (m * 3 + n) * 256, __builtin_bit_cast(u32x4, kacc[m][n]));
    WAIT_VM(0);
  }
  BARRIER();
  gu32* cnt = (gu32*)(ws + OFF_CNT + j * 256);
  if (tid == 0) __hip_atomic_fetch_add(cnt, 1u, __ATOMIC_RELAXED, __HIP_MEMORY_SCOPE_AGENT);
  {
    u16* pq = (u16*)(ws + OFF_PHIQ);
    u32x4 phi[5];
    feature_map(acc[0], smem + K1_PHI + lane * 16, phi);
#pragma unroll
    for (int i = 0; i < 5; ++i) st16_wt(pq + (((size_t)(tt * 8 + j) * 5 + i) * 64 + lane) * 8, phi[i]);
  }
  if (wave == 0) {
    for (unsigned spins = 0; spins < (1u << 22); ++spins) {
      if (__builtin_amdgcn_readfirstlane(__hip_atomic_load(cnt, __ATOMIC_RELAXED, __HIP_MEMORY_SCOPE_AGENT)) >= 32u) break;
      __builtin_amdgcn_s_sleep(1);
    }
  }
  BARRIER();
  {
    const int pos = tid % 48, pg = tid / 48;
    f32x4 sum = {0.f, 0.f, 0.f, 0.f};
    if (tid < 192) {
      const float* src = (const float*)(ws + OFF_PK) + ((size_t)(j * 32 + pg * 8) * 1536 + mt * 48 + pos) * 4;
      f32x4 v[8];
#pragma unroll
      for (int i = 0; i < 8; ++i) asm volatile("global_load_dwordx4 %0, %1, off sc1" : "=v"(v[i]) : "v"(src + (size_t)i * 6144));
      asm volatile("s_waitcnt vmcnt(0)" : "+v"(v[0]), "+v"(v[1]), "+v"(v[2]), "+v"(v[3]), "+v"(v[4]), "+v"(v[5]), "+v"(v[6]), "+v"(v[7]) :: "memory");
      sum = ((v[0] + v[1]) + (v[2] + v[3])) + ((v[4] + v[5]) + (v[6] + v[7]));
      *(f32x4*)(smem + tid * 16) = sum;
    }
    BARRIER();
    if (tid < 48) {
      const f32x4* r = (const f32x4*)smem + tid;
      sum = (r[0] + r[48]) + (r[96] + r[144]);
      const int q = mt * 48 + tid;
      const int hd = q / 384, mn = (q % 384) >> 6, ln = q & 63, m = mn / 3, n = mn % 3;
      const int f = n * 16 + (ln & 15), d0 = m * 16 + (ln >> 4) * 4;
      u16* kvb = (u16*)(ws + OFF_KV) + (size_t)(4 * j + hd) * 800 + d0 * 40 + f;
      if (f < 36) {
#pragma unroll
        for (int r2 = 0; r2 < 4; ++r2) if (d0 + r2 < 19) kvb[r2 * 40] = (u16)(pk2(sum[r2], 0.f) & 0xffffu);
      }
    }
  }
}

constexpr int K2_KV = 36864, K2_ZERO = K2_KV + 51200, K2_ST = K2_ZERO + 1600, K2_LDS = K2_ST + 9 * 32 * 8 + 32 * 8;

__global__ __launch_bounds__(576) void k2_attn(const u16* __restrict__ x, char* __restrict__ ws) {
  extern __shared__ __attribute__((aligned(16))) char smem[];
  const int tid = threadIdx.x, lane = tid & 63;
  const int wave = __builtin_amdgcn_readfirstlane(tid >> 6);
  const int b = blockIdx.x, xcd = b & 7, idx = b >> 3;
  const int rt = xcd * 8 + (idx >> 2), cb = idx & 3;
  const int g = lane >> 4, t = lane & 15;
  const int nt = cb * 9 + wave;
  {
    const unsigned lds0 = (unsigned)(size_t)smem;
    const char* ks = ws + OFF_KV + wave * 6144;
#pragma unroll
    for (int i = 0; i < 2; ++i) glds16((unsigned)lane * 16u, ks + i * 1024, lds0 + K2_KV + wave * 6144 + i * 1024);
    if (wave < 8) {
#pragma unroll
      for (int i = 2; i < 6; ++i) glds16((unsigned)lane * 16u, ks + i * 1024, lds0 + K2_KV + wave * 6144 + i * 1024);
    }
    if (tid < 100) *(u32x4*)(smem + K2_ZERO + tid * 16) = (u32x4){0u, 0u, 0u, 0u};
  }
  bf16x8 ph[2][5];
  {
    const int u = wave & 7;
    const u16* pq = (const u16*)(ws + OFF_PHIQ);
#pragma unroll
    for (int a = 0; a < 2; ++a)
#pragma unroll
      for (int s = 0; s < 5; ++s) ph[a][s] = *(const bf16x8*)(pq + (((size_t)((rt * 2 + a) * 8 + u) * 5 + s) * 64 + lane) * 8);
  }
  bf16x8 wf[18];
  const u16* wp = (const u16*)(ws + OFF_WO) + ((size_t)nt * 18 * 64 + lane) * 8;
  WAIT_VM(10);
  BARRIER();
  if (wave < 8) {
    const int u = wave;
    f32x4 oacc[2][5];
#pragma unroll
    for (int a = 0; a < 2; ++a)
#pragma unroll
      for (int i = 0; i < 5; ++i) oacc[a][i] = (f32x4){0.f, 0.f, 0.f, 0.f};
    const char* kb = smem + (((t >> 2) == g) ? K2_KV + (4 * u + g) * 1600 + (t & 3) * 80 : K2_ZERO);
#pragma unroll
    for (int s = 0; s < 5; ++s) {
#pragma unroll
      for (int i = 0; i < 5; ++i) {
        const bf16x8 a8 = *(const bf16x8*)(kb + i * 320 + s * 16);
        oacc[0][i] = MFMA16(a8, ph[0][s], oacc[0][i]);
        oacc[1][i] = MFMA16(a8, ph[1][s], oacc[1][i]);
      }
      if (s >= 1 && s <= 3) {
#pragma unroll
        for (int q = 0; q < 6; ++q) wf[(s - 1) * 6 + q] = *(const bf16x8*)(wp + (size_t)((s - 1) * 6 + q) * 512);
      }
      __builtin_amdgcn_sched_barrier(0);
    }
#pragma unroll
    for (int a = 0; a < 2; ++a) {
      const float den = rbf(rbf(oacc[a][4][2]) + 1e-6f), rden = __builtin_amdgcn_rcpf(den);
#pragma unroll
      for (int d = 0; d < 18; d += 2) {
        unsigned nq = pk2(oacc[a][d >> 2][d & 3], oacc[a][d >> 2][(d & 3) + 1]);
        unsigned o = pk2(fdiv(lo2f(nq), den, rden), fdiv(hi2f(nq), den, rden));
        const int pos = 18 * u + d;
        *(unsigned*)(smem + ((a * 18 + (pos >> 3)) * 64 + lane) * 16 + (pos & 7) * 2) = o;
      }
    }
  }
  else {
#pragma unroll
    for (int ks = 0; ks < 18; ++ks) wf[ks] = *(const bf16x8*)(wp + (size_t)ks * 512);
  }
  const float* mf = (const float*)(ws + OFF_MODF);
  const int n0 = nt * 16 + 4 * g;
  const f32x4 ga = *(const f32x4*)(mf + 2 * 576 + n0), sb = *(const f32x4*)(mf + 3 * 576 + n0), c1b = *(const f32x4*)(mf + 4 * 576 + n0);
  u32x2 xv[2];
#pragma unroll
  for (int a = 0; a < 2; ++a) xv[a] = *(const u32x2*)(x + (size_t)((rt * 2 + a) * 16 + t) * D_ + n0);
  __syncthreads();
  f32x4 acc[2] = {{0.f, 0.f, 0.f, 0.f}, {0.f, 0.f, 0.f, 0.f}}, acc2[2] = {{0.f, 0.f, 0.f, 0.f}, {0.f, 0.f, 0.f, 0.f}};
#pragma unroll
  for (int ks = 0; ks < 18; ks += 2)
#pragma unroll
    for (int a = 0; a < 2; ++a) {
      bf16x8 of = *(const bf16x8*)(smem + ((a * 18 + ks) * 64 + lane) * 16);
      bf16x8 of2 = *(const bf16x8*)(smem + ((a * 18 + ks + 1) * 64 + lane) * 16);
      acc[a] = MFMA16(wf[ks], of, acc[a]);
      acc2[a] = MFMA16(wf[ks + 1], of2, acc2[a]);
    }
  acc[0] += acc2[0]; acc[1] += acc2[1];
  float x1[2][4];
  float* st = (float*)(smem + K2_ST);
  float* fin = st + 9 * 32 * 2;
#pragma unroll
  for (int a = 0; a < 2; ++a) {
    unsigned a01 = pk2(acc[a][0], acc[a][1]), a23 = pk2(acc[a][2], acc[a][3]);
    unsigned y01 = pk2(ga[0] * lo2f(a01), ga[1] * hi2f(a01)), y23 = pk2(ga[2] * lo2f(a23), ga[3] * hi2f(a23));
    unsigned z01 = pk2(lo2f(xv[a][0]) + lo2f(y01), hi2f(xv[a][0]) + hi2f(y01));
    unsigned z23 = pk2(lo2f(xv[a][1]) + lo2f(y23), hi2f(xv[a][1]) + hi2f(y23));
    x1[a][0] = lo2f(z01); x1[a][1] = hi2f(z01); x1[a][2] = lo2f(z23); x1[a][3] = hi2f(z23);
    st8_wt((u16*)(ws + OFF_X1) + (((size_t)(rt * 2 + a) * 36 + nt) * 64 + lane) * 4, (u32x2){z01, z23});
    float s = (x1[a][0] + x1[a][1]) + (x1[a][2] + x1[a][3]);
    s = sum_g4(s);
    const float mw = s * (1.f / 16.f);
    float q = 0.f;
#pragma unroll
    for (int r = 0; r < 4; ++r) { float d = x1[a][r] - mw; q += d * d; }
    q = sum_g4(q);
    if (g == 0) *(f32x2*)(st + (wave * 32 + a * 16 + t) * 2) = (f32x2){mw, q};
  }
  __syncthreads();
  if (wave == 0) {
    const int tok = lane & 31;
    float mw[9], s = 0.f, q = 0.f;
#pragma unroll
    for (int w = 0; w < 9; ++w) { f32x2 v = *(const f32x2*)(st + (w * 32 + tok) * 2); mw[w] = v[0]; s += v[0]; q += v[1]; }
    const float mb = s * (1.f / 9.f);
    float dq = 0.f;
#pragma unroll
    for (int w = 0; w < 9; ++w) { float d = mw[w] - mb; dq += d * d; }
    q += 16.f * dq;
    const float mine = lane < 32 ? mb : q;
    gu64* G = (gu64*)(ws + OFF_STAT) + (size_t)rt * 4 * 64;
    __hip_atomic_store(G + cb * 64 + lane, (1ull << 32) | (unsigned long long)__float_as_uint(mine), __ATOMIC_RELAXED, __HIP_MEMORY_SCOPE_AGENT);
    float vals[4];
    for (unsigned spins = 0; spins < (1u << 22); ++spins) {
      bool ok = true;
#pragma unroll
      for (int c = 0; c < 4; ++c) {
        unsigned long long v = __hip_atomic_load(G + c * 64 + lane, __ATOMIC_RELAXED, __HIP_MEMORY_SCOPE_AGENT);
        vals[c] = __uint_as_float((unsigned)v);
        ok &= (v >> 32) == 1ull;
      }
      if (__all(ok)) break;
      __builtin_amdgcn_s_sleep(1);
    }
    float m2 = (vals[0] + vals[1]) + (vals[2] + vals[3]);
    const float mean = __shfl(m2, tok) * 0.25f;
    float dd = 0.f;
#pragma unroll
    for (int c = 0; c < 4; ++c) { float d = vals[c] - mean; dd += d * d; }
    const float M2 = __shfl(m2, 32 + tok) + 144.f * dd;
    if (lane < 32) {
      const float mbf = rbf(mean), sd = rbf(sqrtf(rbf(rbf(M2 * (1.f / 576.f)) + 1e-5f)));
      *(f32x2*)(fin + lane * 2) = (f32x2){mbf, sd};
    }
  }
  __syncthreads();
#pragma unroll
  for (int a = 0; a < 2; ++a) {
    const f32x2 ms = *(const f32x2*)(fin + (a * 16 + t) * 2);
    const float rsd = 1.f / ms[1];
    unsigned d01 = pk2(x1[a][0] - ms[0], x1[a][1] - ms[0]), d23 = pk2(x1[a][2] - ms[0], x1[a][3] - ms[0]);
    unsigned n01 = pk2(fdiv(lo2f(d01), ms[1], rsd), fdiv(hi2f(d01), ms[1], rsd));
    unsigned n23 = pk2(fdiv(lo2f(d23), ms[1], rsd), fdiv(hi2f(d23), ms[1], rsd));
    unsigned m01 = pk2(lo2f(n01) * c1b[0], hi2f(n01) * c1b[1]), m23 = pk2(lo2f(n23) * c1b[2], hi2f(n23) * c1b[3]);
    unsigned h01 = pk2(lo2f(m01) + sb[0], hi2f(m01) + sb[1]), h23 = pk2(lo2f(m23) + sb[2], hi2f(m23) + sb[3]);
    st8_wt((u16*)(ws + OFF_H2) + (((size_t)rt * 36 + nt) * 64 + (g & 1) * 32 + a * 16 + t) * 8 + (g >> 1) * 4, (u32x2){h01, h23});
  }
}

constexpr int K3_UOFF = 9216 + 128, K3_AOFF = 18688, K3_SLOT = K3_AOFF + 16384;

DI float geglu(float gv, float uv) {
  const float k0 = -2.302208198f;
  const float k1 = -0.1029432397f;
  float tq = gv * gv;
  float pz = __builtin_fmaf(tq, k1, k0);
  float e = __builtin_amdgcn_exp2f(gv * pz);
  float r = __builtin_amdgcn_rcpf(1.f + e);
  return gv * r * uv;
}

constexpr int K4_SLOT = 18432 + 16384;

__global__ __launch_bounds__(512, 2) void k3_ffn(const u16* __restrict__ wg, const u16* __restrict__ wu, const u16* __restrict__ wd, char* __restrict__ ws, u16* __restrict__ out) {
  extern __shared__ __attribute__((aligned(16))) char smem[];
  const int tid = threadIdx.x, lane = tid & 63;
  const int wave = __builtin_amdgcn_readfirstlane(tid >> 6);
  const int b = blockIdx.x, xcd = b & 7, idx = b >> 3;
  {
  const int ntile = xcd * 4 + (idx & 3), mtile = idx >> 2;
  const unsigned lds0 = (unsigned)(size_t)smem;
  const int T = mtile * 8 + wave;
  const int gl0 = wave * 64 + lane, gl1 = (wave == 0 ? 512 : (wave - 1) * 64) + lane, gl2 = 448 + wave * 16 + (lane & 15);
  const unsigned vo0 = (unsigned)((gl0 / 18) * (DF_ * 2) + (gl0 % 18) * 16);
  const unsigned vo1 = (unsigned)((gl1 / 18) * (DF_ * 2) + (gl1 % 18) * 16);
  const unsigned vo2 = (unsigned)((gl2 / 18) * (DF_ * 2) + (gl2 % 18) * 16);
  const unsigned voa = (unsigned)lane * 16u;
  const char* sg = (const char*)wg + ntile * 288;
  const char* su = (const char*)wu + ntile * 288;
  const char* s1 = wave == 0 ? sg : su;
  const char* sa = ws + OFF_H2 + (size_t)T * 36 * 1024;
  const unsigned d0 = lds0 + wave * 1024, d1 = lds0 + (wave == 0 ? 8192 : K3_UOFF + (wave - 1) * 1024);
  const unsigned d2 = lds0 + K3_UOFF + 7168 + wave * 256, d3 = lds0 + K3_AOFF + wave * 2048;
  auto piece = [&](int st, int i) {
    const size_t dl = (size_t)st * 32 * (DF_ * 2);
    const unsigned sl = (st & 3) * K3_SLOT;
    if (i == 0) glds16(vo0, sg + dl, d0 + sl);
    else if (i == 1) glds16(vo1, s1 + dl, d1 + sl);
    else if (i == 2) glds16q(vo2, su + dl, d2 + sl);
    else if (i == 3) glds16(voa, sa + (size_t)st * 2048, d3 + sl);
    else glds16(voa, sa + (size_t)st * 2048 + 1024, d3 + sl + 1024);
  };
#pragma unroll
  for (int st = 0; st < 3; ++st)
#pragma unroll
    for (int i = 0; i < 5; ++i) piece(st, i);
  f32x16 acc[9];
#pragma unroll
  for (int i = 0; i < 9; ++i)
#pragma unroll
    for (int r = 0; r < 16; ++r) acc[i][r] = 0.f;
  const int h = lane >> 5, grp = (lane >> 4) & 1, q4 = (lane & 15) >> 2, p = lane & 3;
  const unsigned rbase = lds0 + (grp ? K3_UOFF : 0) + (4 * h + q4) * 288 + p * 8;
  const unsigned abase = K3_AOFF + wave * 2048 + lane * 16;
  bf16x8 wA[9], wB[9], aA, aB;
#pragma unroll
  for (int i = 0; i < 9; ++i) wB[i] = (bf16x8){0, 0, 0, 0, 0, 0, 0, 0};
  aB = (bf16x8){0, 0, 0, 0, 0, 0, 0, 0};
  auto body = [&](int st, bool pf) __attribute__((always_inline)) {
    BARRIER();
    const unsigned sl = (st & 3) * K3_SLOT;
    aA = *(const bf16x8*)(smem + sl + abase);
#pragma unroll
    for (int tn = 0; tn < 9; ++tn) {
      const unsigned ad = rbase + sl + tn * 32;
      wA[tn] = tr_frag(ad, ad + 8 * 288);
      acc[tn] = MFMA32(wB[tn], aB, acc[tn]);
      if (pf && (tn == 1 || tn == 4 || tn == 7)) piece(st + 3, tn / 3);
    }
    __builtin_amdgcn_sched_group_barrier(0x100, 3, 0);
#pragma unroll
    for (int tn = 0; tn < 8; ++tn) { __builtin_amdgcn_sched_group_barrier(0x8, 1, 0); __builtin_amdgcn_sched_group_barrier(0x100, 2, 0); }
    __builtin_amdgcn_sched_group_barrier(0x8, 1, 0);
    __builtin_amdgcn_sched_barrier(0);
    aB = *(const bf16x8*)(smem + sl + abase + 1024);
#pragma unroll
    for (int tn = 0; tn < 9; ++tn) {
      const unsigned ad = rbase + sl + 16 * 288 + tn * 32;
      wB[tn] = tr_frag(ad, ad + 8 * 288);
      acc[tn] = MFMA32(wA[tn], aA, acc[tn]);
      if (pf && (tn == 1 || tn == 4)) piece(st + 3, 3 + tn / 3);
    }
    __builtin_amdgcn_sched_group_barrier(0x100, 3, 1);
#pragma unroll
    for (int tn = 0; tn < 8; ++tn) { __builtin_amdgcn_sched_group_barrier(0x8, 1, 1); __builtin_amdgcn_sched_group_barrier(0x100, 2, 1); }
    __builtin_amdgcn_sched_group_barrier(0x8, 1, 1);
    __builtin_amdgcn_sched_barrier(0);
  };
  WAIT_VM(10); body(0, true);
#pragma unroll 1
  for (int st = 1; st < 15; ++st) { WAIT_VM(10); body(st, true); }
  WAIT_VM(10); body(15, false);
  WAIT_VM(5); body(16, false);
  WAIT_VM(0); body(17, false);
#pragma unroll
  for (int tn = 0; tn < 9; ++tn) acc[tn] = MFMA32(wB[tn], aB, acc[tn]);
  u16* hh = (u16*)(ws + OFF_HH);
  const int t32 = lane & 31;
#pragma unroll
  for (int tn = 0; tn < 9; ++tn) {
    u32x4 o;
#pragma unroll
    for (int j = 0; j < 4; ++j) o[j] = pk2(geglu(acc[tn][2 * j], acc[tn][8 + 2 * j]), geglu(acc[tn][2 * j + 1], acc[tn][9 + 2 * j]));
    const int ft = ntile * 9 + tn;
    st16_wt(hh + (((size_t)(2 * T + (t32 >> 4)) * 144 + (ft >> 1)) * 64 + (2 * (ft & 1) + h) * 16 + (t32 & 15)) * 8, o);
  }
  WAIT_VM(0);
  BARRIER();
  if (tid == 0) __hip_atomic_fetch_add((gu32*)(ws + OFF_CNT3 + (mtile * 4 + (ntile >> 3)) * 256), 1u, __ATOMIC_RELAXED, __HIP_MEMORY_SCOPE_AGENT);
  }
  const int sp = xcd >> 1, mtile = (xcd & 1) * 8 + (idx >> 2), nb = idx & 3;
  const int tg = wave & 3, kh = wave >> 2;
  const int g = lane >> 4, q4 = (lane & 15) >> 2, p = lane & 3;
  const unsigned lds0 = (unsigned)(size_t)smem;
  const int tt0 = mtile * 8 + tg * 2;
  const int gl0 = tid, gl1 = 512 + tid, gl2 = 1024 + wave * 16 + (lane & 15);
  const unsigned vo0 = (unsigned)((gl0 / 18) * (D_ * 2) + (gl0 % 18) * 16);
  const unsigned vo1 = (unsigned)((gl1 / 18) * (D_ * 2) + (gl1 % 18) * 16);
  const unsigned vo2 = (unsigned)((gl2 / 18) * (D_ * 2) + (gl2 % 18) * 16);
  const unsigned voa = (unsigned)lane * 16u;
  const char* sw = (const char*)wd + (size_t)(1152 * sp) * (D_ * 2) + nb * 288;
  const char* sh = ws + OFF_HH + ((size_t)tt0 * 144 + 36 * sp + kh) * 1024;
  const unsigned d0 = lds0 + wave * 1024, d2 = lds0 + 16384 + wave * 256, d3 = lds0 + 18432 + wave * 2048;
  auto piece = [&](int st, int i) {
    const size_t dl = (size_t)st * 64 * (D_ * 2);
    const unsigned sl = (st & 3) * K4_SLOT;
    if (i == 0) glds16(vo0, sw + dl, d0 + sl);
    else if (i == 1) glds16(vo1, sw + dl, d0 + sl + 8192);
    else if (i == 2) glds16q(vo2, sw + dl, d2 + sl);
    else if (i == 3) glds16c(voa, sh + (size_t)st * 2048, d3 + sl);
    else glds16c(voa, sh + (size_t)st * 2048 + 144 * 1024, d3 + sl + 1024);
  };
#pragma unroll
  for (int st = 0; st < 3; ++st)
#pragma unroll
    for (int i = 0; i < 3; ++i) piece(st, i);
  if (wave == 0) {
    gu32* c3 = (gu32*)(ws + OFF_CNT3 + ((mtile >> 1) * 4 + sp) * 256);
    for (unsigned spins = 0; spins < (1u << 22); ++spins) {
      if (__builtin_amdgcn_readfirstlane(__hip_atomic_load(c3, __ATOMIC_RELAXED, __HIP_MEMORY_SCOPE_AGENT)) >= 8u) break;
      __builtin_amdgcn_s_sleep(1);
    }
  }
  BARRIER();
#pragma unroll
  for (int st = 0; st < 3; ++st)
#pragma unroll
    for (int i = 3; i < 5; ++i) piece(st, i);
  f32x4 acc[2][9];
#pragma unroll
  for (int a = 0; a < 2; ++a)
#pragma unroll
    for (int i = 0; i < 9; ++i) acc[a][i] = (f32x4){0.f, 0.f, 0.f, 0.f};
  const unsigned rbase = lds0 + (32 * kh + 16 * (g >> 1) + 4 * (g & 1) + q4) * 288 + p * 8;
  const unsigned abase = 18432 + wave * 2048 + lane * 16;
  bf16x8 wA[9], wB[9], hA0, hA1, hB0, hB1;
#pragma unroll
  for (int i = 0; i < 9; ++i) wB[i] = (bf16x8){0, 0, 0, 0, 0, 0, 0, 0};
  hB0 = (bf16x8){0, 0, 0, 0, 0, 0, 0, 0}; hB1 = hB0;
  auto body = [&](int st, bool pf, bf16x8 (&wN)[9], bf16x8& hN0, bf16x8& hN1, bf16x8 (&wO)[9], bf16x8& hO0, bf16x8& hO1) __attribute__((always_inline)) {
    BARRIER();
    const unsigned sl = (st & 3) * K4_SLOT;
    hN0 = *(const bf16x8*)(smem + sl + abase);
    hN1 = *(const bf16x8*)(smem + sl + abase + 1024);
#pragma unroll
    for (int i = 0; i < 9; ++i) {
      const unsigned ad = rbase + sl + i * 32;
      wN[i] = tr_frag(ad, ad + 8 * 288);
      acc[0][i] = MFMA16(wO[i], hO0, acc[0][i]);
      acc[1][i] = MFMA16(wO[i], hO1, acc[1][i]);
      if (pf && (i & 1) == 0) piece(st + 3, i >> 1);
    }
    __builtin_amdgcn_sched_group_barrier(0x100, 4, 0);
#pragma unroll
    for (int i = 0; i < 8; ++i) { __builtin_amdgcn_sched_group_barrier(0x8, 2, 0); __builtin_amdgcn_sched_group_barrier(0x100, 2, 0); }
    __builtin_amdgcn_sched_group_barrier(0x8, 2, 0);
    __builtin_amdgcn_sched_barrier(0);
  };
  WAIT_VM(4); body(0, true, wA, hA0, hA1, wB, hB0, hB1);
  WAIT_VM(7); body(1, true, wB, hB0, hB1, wA, hA0, hA1);
#pragma unroll 1
  for (int st = 2; st < 14; st += 2) {
    WAIT_VM(10); body(st, true, wA, hA0, hA1, wB, hB0, hB1);
    WAIT_VM(10); body(st + 1, true, wB, hB0, hB1, wA, hA0, hA1);
  }
  WAIT_VM(10); body(14, true, wA, hA0, hA1, wB, hB0, hB1);
  WAIT_VM(10); body(15, false, wB, hB0, hB1, wA, hA0, hA1);
  WAIT_VM(5); body(16, false, wA, hA0, hA1, wB, hB0, hB1);
  WAIT_VM(0); body(17, false, wB, hB0, hB1, wA, hA0, hA1);
#pragma unroll
  for (int i = 0; i < 9; ++i) { acc[0][i] = MFMA16(wB[i], hB0, acc[0][i]); acc[1][i] = MFMA16(wB[i], hB1, acc[1][i]); }
  BARRIER();
  {
    char* xb = smem + (((tg * 2 + (1 - kh)) * 9) * 64 + lane) * 16;
#pragma unroll
    for (int i = 0; i < 9; ++i) *(f32x4*)(xb + i * 1024) = kh ? acc[0][i] : acc[1][i];
  }
  BARRIER();
  float* P = (float*)(ws + OFF_P);
  {
    const char* xb = smem + (((tg * 2 + kh) * 9) * 64 + lane) * 16;
    f32x4 r[9];
#pragma unroll
    for (int i = 0; i < 9; ++i) r[i] = (kh ? acc[1][i] : acc[0][i]) + *(const f32x4*)(xb + i * 1024);
    if (tg != sp) {
      const unsigned po = (unsigned)(OFF_P + (((size_t)(sp * 128 + tt0 + kh) * 4 + nb) * 5 * 64 + lane) * 16);
#pragma unroll
      for (int i = 0; i < 5; ++i) {
        u32x4 o = {pk2(r[2 * i][0], r[2 * i][1]), pk2(r[2 * i][2], r[2 * i][3]), 0u, 0u};
        if (i < 4) { o[2] = pk2(r[2 * i + 1][0], r[2 * i + 1][1]); o[3] = pk2(r[2 * i + 1][2], r[2 * i + 1][3]); }
        __builtin_amdgcn_raw_buffer_store_b128(o, WS_RSRC, po + i * 1024, 0, 16);
      }
      WAIT_VM(0);
    } else {
#pragma unroll
      for (int i = 0; i < 9; ++i) *(f32x4*)(smem + 73728 + ((kh * 9 + i) * 64 + lane) * 16) = r[i];
    }
  }
  BARRIER();
  gu32* c4 = (gu32*)(ws + OFF_CNT4 + (mtile * 4 + nb) * 256);
  if (tid == 0) __hip_atomic_fetch_add(c4, 1u, __ATOMIC_RELAXED, __HIP_MEMORY_SCOPE_AGENT);
  {
    const int ttb = mtile * 8 + sp * 2;
    const float* mf = (const float*)(ws + OFF_MODF) + 5 * 576 + nb * 144 + g * 4;
    u32x2 xv[2][2];
    f32x4 gb[2][2];
#pragma unroll
    for (int q = 0; q < 2; ++q) {
      const int e = min(wave + 8 * q, 9), a = e / 5, pi = e % 5;
#pragma unroll
      for (int hh = 0; hh < 2; ++hh) {
        const int i = min(2 * pi + hh, 8);
        xv[q][hh] = *(const u32x2*)((const u16*)(ws + OFF_X1) + (((size_t)(ttb + a) * 36 + nb * 9 + i) * 64 + lane) * 4);
        gb[q][hh] = *(const f32x4*)(mf + i * 16);
      }
    }
    if (wave == 0) {
      for (unsigned spins = 0; spins < (1u << 22); ++spins) {
        if (__builtin_amdgcn_readfirstlane(__hip_atomic_load(c4, __ATOMIC_RELAXED, __HIP_MEMORY_SCOPE_AGENT)) >= 4u) break;
        __builtin_amdgcn_s_sleep(1);
      }
    }
    BARRIER();
    u32x4 pv[2][3];
#pragma unroll
    for (int q = 0; q < 2; ++q) {
      const int e = min(wave + 8 * q, 9), a = e / 5, pi = e % 5;
#pragma unroll
      for (int o = 0; o < 3; ++o) {
        const int so = o + (o >= sp ? 1 : 0);
        pv[q][o] = __builtin_amdgcn_raw_buffer_load_b128(WS_RSRC,
                     (unsigned)(OFF_P + ((((size_t)(so * 128 + ttb + a) * 4 + nb) * 5 + pi) * 64 + lane) * 16), 0, 16);
      }
    }
#pragma unroll
    for (int q = 0; q < 2; ++q) {
      const int e = wave + 8 * q;
      if (e < 10) {
        const int a = e / 5, pi = e % 5;
#pragma unroll
        for (int hh = 0; hh < 2; ++hh) {
          const int i = 2 * pi + hh;
          if (i < 9) {
            const f32x4 own = *(const f32x4*)(smem + 73728 + ((a * 9 + i) * 64 + lane) * 16);
            f32x4 pf[3];
#pragma unroll
            for (int o = 0; o < 3; ++o) pf[o] = (f32x4){lo2f(pv[q][o][2 * hh]), hi2f(pv[q][o][2 * hh]), lo2f(pv[q][o][2 * hh + 1]), hi2f(pv[q][o][2 * hh + 1])};
            const f32x4 p0 = sp == 0 ? own : pf[0];
            const f32x4 p1 = sp == 1 ? own : (sp < 1 ? pf[0] : pf[1]);
            const f32x4 p2 = sp == 2 ? own : (sp < 2 ? pf[1] : pf[2]);
            const f32x4 p3 = sp == 3 ? own : pf[2];
            const f32x4 sm = ((p0 + p1) + p2) + p3;
            unsigned d01 = pk2(sm[0], sm[1]), d23 = pk2(sm[2], sm[3]);
            unsigned y01 = pk2(gb[q][hh][0] * lo2f(d01), gb[q][hh][1] * hi2f(d01)), y23 = pk2(gb[q][hh][2] * lo2f(d23), gb[q][hh][3] * hi2f(d23));
            unsigned z01 = pk2(lo2f(xv[q][hh][0]) + lo2f(y01), hi2f(xv[q][hh][0]) + hi2f(y01));
            unsigned z23 = pk2(lo2f(xv[q][hh][1]) + lo2f(y23), hi2f(xv[q][hh][1]) + hi2f(y23));
            *(u32x2*)(out + (size_t)((ttb + a) * 16 + (lane & 15)) * D_ + nb * 144 + i * 16 + g * 4) = (u32x2){z01, z23};
          }
        }
      }
    }
  }
  asm volatile("cw_text_end:");
}

extern "C" void kernel_launch(void* const* d_in, const int* in_sizes, int n_in, void* d_out, int out_size, void* d_ws, size_t ws_size,
                              hipStream_t stream) {
  const u16* x = (const u16*)d_in[0];
  const u16* wqkv = (const u16*)d_in[1];
  const u16* wphi = (const u16*)d_in[2];
  const u16* wo = (const u16*)d_in[3];
  const u16* wg = (const u16*)d_in[5];
  const u16* wu = (const u16*)d_in[6];
  const u16* wd = (const u16*)d_in[7];
  const u16* c = (const u16*)d_in[8];
  const u16* wmod = (const u16*)d_in[9];
  char* ws = (char*)d_ws;
  (void)hipFuncSetAttribute((const void*)k1_qkv, hipFuncAttributeMaxDynamicSharedMemorySize, K1_LDS);
  (void)hipFuncSetAttribute((const void*)k2_attn, hipFuncAttributeMaxDynamicSharedMemorySize, K2_LDS);
  (void)hipFuncSetAttribute((const void*)k3_ffn, hipFuncAttributeMaxDynamicSharedMemorySize, 4 * K3_SLOT);
  k0_prep<<<K0_BLOCKS, 256, 0, stream>>>(x, wqkv, wphi, wo, c, wmod, ws);
  k1_qkv<<<256, 320, K1_LDS, stream>>>(ws, wg, wu, wd);
  k2_attn<<<256, 576, K2_LDS, stream>>>(x, ws);
  k3_ffn<<<256, 512, 4 * K3_SLOT, stream>>>(wg, wu, wd, ws, (u16*)d_out);
}
```
